```python
import torch

def _softmax(x, axis=-1):
    return torch.softmax(x, dim=axis)
def _gelu(x):
    return torch.nn.functional.gelu(x, approximate="tanh")
def _silu(x):
    return torch.nn.functional.silu(x)
def _sigmoid(x):
    return torch.sigmoid(x)
def _layernorm(x, axis=-1, eps=1e-5):
    m = x.mean(dim=axis, keepdim=True)
    v = x.var(dim=axis, keepdim=True, unbiased=False)
    return (x - m) / torch.sqrt(v + eps)
def _scatter_add(tgt, idx, src, axis=0):
    return tgt.clone().index_add_(axis, idx, src)
def _segment_sum(src, seg, n):
    out = torch.zeros((n,) + src.shape[1:], dtype=src.dtype)
    return out.index_add_(0, seg, src)
def _pad(x, pw):
    flat = tuple(v for pair in reversed(pw) for v in pair)
    return torch.nn.functional.pad(x, flat)

kernel_name = 'gated_mix__ssm_conv_s4__dense_activation_relu__prompt'

def setup_inputs():

    _w = (lambda a: (lambda t: t.to(torch.bfloat16) if t.is_floating_point() else t)(torch.as_tensor(a)))
    def _A_setup():
        B, L, D, N = 1, 2048, 512, 128
        A_re = -(lambda _x: torch.exp(torch.as_tensor(_x)))((lambda *s: torch.randn(*s, dtype=torch.bfloat16) * 3.2)(D, N))
        A_im = torch.pi * (lambda *s: torch.randn(*s, dtype=torch.bfloat16) * 0.2)(D, N)
        B_re = (lambda *s: torch.randn(*s, dtype=torch.bfloat16) * 0.4)(D, N)
        B_im = (lambda *s: torch.randn(*s, dtype=torch.bfloat16) * 0.4)(D, N)
        C_re = (lambda *s: torch.randn(*s, dtype=torch.bfloat16) * 0.4)(D, N)
        C_im = (lambda *s: torch.randn(*s, dtype=torch.bfloat16) * 0.4)(D, N)
        log_dt = (lambda *s: torch.randn(*s, dtype=torch.bfloat16) * 0.2)(D) * 0.5 - 4.0
        Dskip = (lambda *s: torch.randn(*s, dtype=torch.bfloat16) * 0.4)(D)
        return {
            "u": _w((lambda *s: torch.randn(*s, dtype=torch.bfloat16) * 0.05)(B, L, D)),
            "A_re": _w(A_re),
            "A_im": _w(A_im),
            "B_re": _w(B_re),
            "B_im": _w(B_im),
            "C_re": _w(C_re),
            "C_im": _w(C_im),
            "log_dt": _w(log_dt),
            "Dskip": _w(Dskip),
        }
    _A_in = _A_setup()

    def _B_setup():
        B, S, D, Dff = 1, 2048, 512, 1792
        return {
            "x": _w((lambda *s: torch.randn(*s, dtype=torch.bfloat16) * 0.05)(B, S, D)),
            "w1": _w((lambda *s: torch.randn(*s, dtype=torch.bfloat16) * 0.05)(D, Dff)),
            "w2": _w((lambda *s: torch.randn(*s, dtype=torch.bfloat16) * 0.05)(Dff, D)),
        }
    _B_in = _B_setup()

    return {
        "A_u": _A_in["u"],
        "A_A_re": _A_in["A_re"],
        "A_A_im": _A_in["A_im"],
        "A_B_re": _A_in["B_re"],
        "A_B_im": _A_in["B_im"],
        "A_C_re": _A_in["C_re"],
        "A_C_im": _A_in["C_im"],
        "A_log_dt": _A_in["log_dt"],
        "A_Dskip": _A_in["Dskip"],
        "B_x": _B_in["x"],
        "B_w1": _B_in["w1"],
        "B_w2": _B_in["w2"],
        "gate_w": _w((lambda *s: torch.randn(*s, dtype=torch.bfloat16) * 0.05)(512) * 16.0),
    }

def reference(A_u, A_A_re, A_A_im, A_B_re, A_B_im, A_C_re, A_C_im, A_log_dt, A_Dskip, B_x, B_w1, B_w2, gate_w):
    def _A_ref(u, A_re, A_im, B_re, B_im, C_re, C_im, log_dt, Dskip):
        Bsz, L, D = u.shape
        dt = torch.exp(log_dt)[:, None]
        A = A_re + 1j * A_im
        Bc = B_re + 1j * B_im
        Cc = C_re + 1j * C_im
        logdA = dt * A
        dB = dt * Bc
        l = (lambda x, dt: x.to(dt))(torch.arange(L), A_re.dtype)
        P = torch.exp(logdA[:, :, None] * l[None, None, :])
        K = torch.einsum("dn,dnl,dn->dl", Cc, P, dB).real
        L2 = 2 * L
        Kp = _pad(K, ((0, 0), (0, L)))
        up = (lambda x, *d: x.permute(*d))(u, 0, 2, 1)
        up = _pad(up, ((0, 0), (0, 0), (0, L)))
        Kf = (lambda x, axes=(-2,-1): torch.fft.rfft2(x.float(), dim=axes))(Kp[:, None, :], axes=(-2, -1))
        Uf = (lambda x, axes=(-2,-1): torch.fft.rfft2(x.float(), dim=axes))(up[:, :, None, :], axes=(-2, -1))
        Yf = Uf * Kf[None, :, :, :]
        y = (lambda x, s, axes=(-2,-1): torch.fft.irfft2(x, s=s, dim=axes).to(torch.bfloat16))(Yf, (1, L2), axes=(-2, -1))[:, :, 0, :L]
        y = (lambda x, *d: x.permute(*d))(y, 0, 2, 1) + u * Dskip[None, None, :]
        return y

    def _B_ref(x, w1, w2):
        h = torch.einsum("bsd,df->bsf", x, w1)
        zero = torch.zeros_like(h)
        h = torch.maximum(h, zero)
        return torch.einsum("bsf,fd->bsd", h, w2)

    x = A_u
    g = _sigmoid((x * gate_w).sum(-1, keepdims=True))
    a = _A_ref(x, A_A_re, A_A_im, A_B_re, A_B_im, A_C_re, A_C_im, A_log_dt, A_Dskip)
    b = _B_ref(x, B_w1, B_w2)
    return g * a + (1 - g) * b
```

```cpp
#include <hip/hip_runtime.h>
#include <hip/hip_bf16.h>
#include <stdint.h>

typedef unsigned short u16;
__device__ __forceinline__ float bf2f(u16 x){ return __uint_as_float(((unsigned)x)<<16); }
__device__ __forceinline__ u16 f2bf(float f){ __hip_bfloat16 b = __float2bfloat16(f); return *reinterpret_cast<u16*>(&b); }
__device__ __forceinline__ float rbf(float f){ return bf2f(f2bf(f)); }

constexpr int L=2048, D=512, N=128, DFF=1792;

__global__ void k_gate(const u16* u, const u16* gw, float* g){
  int l = blockIdx.x; int t = threadIdx.x;
  float s=0.f;
  for(int d=t; d<D; d+=64) s += rbf(bf2f(u[l*D+d])*bf2f(gw[d]));
  for(int o=32;o>0;o>>=1) s += __shfl_down(s,o);
  if(t==0){ float z = rbf(s); float sg = 1.f/(1.f+expf(-z)); g[l]=rbf(sg); }
}

__global__ void k_kgen(const u16* Are,const u16* Aim,const u16* Bre,const u16* Bim,const u16* Cre,const u16* Cim,const u16* logdt,float* K){
  int d = blockIdx.x;
  __shared__ float zr[N], zi[N], wr[N], wi[N];
  float dt = rbf(expf(bf2f(logdt[d])));
  for(int n=threadIdx.x;n<N;n+=blockDim.x){
    zr[n]=dt*bf2f(Are[d*N+n]); zi[n]=dt*bf2f(Aim[d*N+n]);
    float br=dt*bf2f(Bre[d*N+n]), bi=dt*bf2f(Bim[d*N+n]);
    float cr=bf2f(Cre[d*N+n]), ci=bf2f(Cim[d*N+n]);
    wr[n]=cr*br-ci*bi; wi[n]=cr*bi+ci*br;
  }
  __syncthreads();
  for(int l=threadIdx.x;l<L;l+=blockDim.x){
    float lf = rbf((float)l);
    float s=0.f;
    for(int n=0;n<N;n++){
      float e=expf(zr[n]*lf); float sn,cs; sincosf(zi[n]*lf,&sn,&cs);
      s += e*(wr[n]*cs - wi[n]*sn);
    }
    K[d*L+l]=s;
  }
}

__global__ void k_conv(const u16* u,const float* K,const u16* Dskip,u16* a){
  int d = blockIdx.x;
  __shared__ float ks[L], us[L];
  for(int l=threadIdx.x;l<L;l+=blockDim.x){ ks[l]=K[d*L+l]; us[l]=bf2f(u[l*D+d]); }
  __syncthreads();
  float dsk=bf2f(Dskip[d]);
  for(int l=threadIdx.x;l<L;l+=blockDim.x){
    float s=0.f;
    for(int j=0;j<=l;j++) s += ks[l-j]*us[j];
    float y=rbf(s);
    float sk=rbf(us[l]*dsk);
    a[l*D+d]=f2bf(y+sk);
  }
}

__global__ void k_gemm1(const u16* x,const u16* w1,u16* h){
  int l=blockIdx.x; __shared__ float xs[D];
  for(int i=threadIdx.x;i<D;i+=blockDim.x) xs[i]=bf2f(x[l*D+i]);
  __syncthreads();
  for(int f=threadIdx.x;f<DFF;f+=blockDim.x){
    float s=0.f;
    for(int k=0;k<D;k++) s+=xs[k]*bf2f(w1[k*DFF+f]);
    float r=rbf(s); r = r>0.f? r:0.f;
    h[l*DFF+f]=f2bf(r);
  }
}
__global__ void k_gemm2(const u16* h,const u16* w2,const u16* a,const float* g,u16* out){
  int l=blockIdx.x; __shared__ float hs[DFF];
  for(int i=threadIdx.x;i<DFF;i+=blockDim.x) hs[i]=bf2f(h[l*DFF+i]);
  __syncthreads();
  float gg=g[l]; float og=rbf(1.f-gg);
  for(int d=threadIdx.x;d<D;d+=blockDim.x){
    float s=0.f;
    for(int k=0;k<DFF;k++) s+=hs[k]*bf2f(w2[k*D+d]);
    float b=rbf(s);
    float t1=rbf(gg*bf2f(a[l*D+d])); float t2=rbf(og*b);
    out[l*D+d]=f2bf(t1+t2);
  }
}

extern "C" void kernel_launch(void* const* d_in, const int* in_sizes, int n_in,
                              void* d_out, int out_size, void* d_ws, size_t ws_size,
                              hipStream_t stream) {
  const u16* u=(const u16*)d_in[0];
  const u16* Are=(const u16*)d_in[1]; const u16* Aim=(const u16*)d_in[2];
  const u16* Bre=(const u16*)d_in[3]; const u16* Bim=(const u16*)d_in[4];
  const u16* Cre=(const u16*)d_in[5]; const u16* Cim=(const u16*)d_in[6];
  const u16* logdt=(const u16*)d_in[7]; const u16* Dskip=(const u16*)d_in[8];
  const u16* x=(const u16*)d_in[9]; const u16* w1=(const u16*)d_in[10]; const u16* w2=(const u16*)d_in[11];
  const u16* gw=(const u16*)d_in[12];
  u16* out=(u16*)d_out;
  char* ws=(char*)d_ws;
  float* g=(float*)ws; ws+=L*4;
  float* K=(float*)ws; ws+=(size_t)D*L*4;
  u16* a=(u16*)ws; ws+=(size_t)L*D*2;
  u16* h=(u16*)ws; ws+=(size_t)L*DFF*2;
  k_gate<<<L,64,0,stream>>>(u,gw,g);
  k_kgen<<<D,256,0,stream>>>(Are,Aim,Bre,Bim,Cre,Cim,logdt,K);
  k_conv<<<D,256,0,stream>>>(u,K,Dskip,a);
  k_gemm1<<<L,256,0,stream>>>(u,w1,h);
  k_gemm2<<<L,256,0,stream>>>(h,w2,a,g,out);
}
```

```cpp
#include <hip/hip_runtime.h>
#include <hip/hip_bf16.h>
#include <stdint.h>

typedef unsigned short u16;
__device__ __forceinline__ float bf2f(u16 x){ return __uint_as_float(((unsigned)x)<<16); }
__device__ __forceinline__ u16 f2bf(float f){ __hip_bfloat16 b = __float2bfloat16(f); return *reinterpret_cast<u16*>(&b); }
__device__ __forceinline__ float rbf(float f){ return bf2f(f2bf(f)); }

constexpr int L=2048, D=512, N=128, DFF=1792;

__global__ void k_gate(const u16* u, const u16* gw, float* g){
  int l = blockIdx.x; int t = threadIdx.x;
  float s=0.f;
  for(int d=t; d<D; d+=64) s += rbf(bf2f(u[l*D+d])*bf2f(gw[d]));
  for(int o=32;o>0;o>>=1) s += __shfl_down(s,o);
  if(t==0){ float z = rbf(s); float sg = 1.f/(1.f+expf(-z)); g[l]=rbf(sg); }
}

__global__ void k_kgen(const u16* Are,const u16* Aim,const u16* Bre,const u16* Bim,const u16* Cre,const u16* Cim,const u16* logdt,float* K){
  int d = blockIdx.x;
  __shared__ float zr[N], zi[N], wr[N], wi[N];
  float dt = rbf(expf(bf2f(logdt[d])));
  for(int n=threadIdx.x;n<N;n+=blockDim.x){
    zr[n]=dt*bf2f(Are[d*N+n]); zi[n]=dt*bf2f(Aim[d*N+n]);
    float br=dt*bf2f(Bre[d*N+n]), bi=dt*bf2f(Bim[d*N+n]);
    float cr=bf2f(Cre[d*N+n]), ci=bf2f(Cim[d*N+n]);
    wr[n]=cr*br-ci*bi; wi[n]=cr*bi+ci*br;
  }
  __syncthreads();
  for(int l=threadIdx.x;l<L;l+=blockDim.x){
    float lf = rbf((float)l);
    float s=0.f;
    for(int n=0;n<N;n++){
      float e=expf(zr[n]*lf); float sn,cs; sincosf(zi[n]*lf,&sn,&cs);
      s += e*(wr[n]*cs - wi[n]*sn);
    }
    K[d*L+l]=s;
  }
}

__global__ void k_conv(const u16* u,const float* K,const u16* Dskip,u16* a){
  int d = blockIdx.x;
  __shared__ float ks[L], us[L];
  for(int l=threadIdx.x;l<L;l+=blockDim.x){ ks[l]=K[d*L+l]; us[l]=bf2f(u[l*D+d]); }
  __syncthreads();
  float dsk=bf2f(Dskip[d]);
  for(int l=threadIdx.x;l<L;l+=blockDim.x){
    float s=0.f;
    for(int j=0;j<=l;j++) s += ks[l-j]*us[j];
    float y=rbf(s);
    float sk=rbf(us[l]*dsk);
    a[l*D+d]=f2bf(y+sk);
  }
}

__global__ void k_gemm1(const u16* x,const u16* w1,u16* h){
  int l=blockIdx.x; __shared__ float xs[D];
  for(int i=threadIdx.x;i<D;i+=blockDim.x) xs[i]=bf2f(x[l*D+i]);
  __syncthreads();
  for(int f=threadIdx.x;f<DFF;f+=blockDim.x){
    float s=0.f;
    for(int k=0;k<D;k++) s+=xs[k]*bf2f(w1[k*DFF+f]);
    float r=rbf(s); r = r>0.f? r:0.f;
    h[l*DFF+f]=f2bf(r);
  }
}
__global__ void k_gemm2(const u16* h,const u16* w2,const u16* a,const float* g,u16* out){
  int l=blockIdx.x; __shared__ float hs[DFF];
  for(int i=threadIdx.x;i<DFF;i+=blockDim.x) hs[i]=bf2f(h[l*DFF+i]);
  __syncthreads();
  float gg=g[l]; float og=rbf(1.f-gg);
  for(int d=threadIdx.x;d<D;d+=blockDim.x){
    float s=0.f;
    for(int k=0;k<DFF;k++) s+=hs[k]*bf2f(w2[k*D+d]);
    float b=rbf(s);
    float t1=rbf(gg*bf2f(a[l*D+d])); float t2=rbf(og*b);
    out[l*D+d]=f2bf(t1+t2);
  }
}

#define PROBE 0

#define R8(x) x x x x x x x x
#define R64(x) R8(R8(x))
#define R512(x) R8(R64(x))
__global__ void k_probe(const char* p, unsigned stride, unsigned* res){
  unsigned long long t0,t1; unsigned v=0;
  const char* q=p+(size_t)blockIdx.x*stride;
  asm volatile("s_memtime %0\n\ts_waitcnt lgkmcnt(0)":"=s"(t0)::"memory");
  asm volatile("global_load_dword %0, %1, off\n\ts_waitcnt vmcnt(0)":"=v"(v):"v"(q):"memory");
  asm volatile("s_memtime %0\n\ts_waitcnt lgkmcnt(0)":"=s"(t1)::"memory");
  if(threadIdx.x==0) res[blockIdx.x]=(unsigned)(t1-t0)+(v&0);
}
__global__ void k_spin(const unsigned* res){
  unsigned v=res[threadIdx.x];
  __shared__ unsigned s[256]; s[threadIdx.x]=v; __syncthreads();
  if(threadIdx.x==0){ unsigned long long sum=0; for(int i=0;i<256;i++) sum+=s[i]; unsigned long long n=sum/256*100;
    n=0;
    unsigned long long t0=__builtin_amdgcn_s_memrealtime();
    while(__builtin_amdgcn_s_memrealtime()-t0 < n/24) {}
  }
}

extern "C" void kernel_launch(void* const* d_in, const int* in_sizes, int n_in,
                              void* d_out, int out_size, void* d_ws, size_t ws_size,
                              hipStream_t stream) {
  const u16* u=(const u16*)d_in[0];
  const u16* Are=(const u16*)d_in[1]; const u16* Aim=(const u16*)d_in[2];
  const u16* Bre=(const u16*)d_in[3]; const u16* Bim=(const u16*)d_in[4];
  const u16* Cre=(const u16*)d_in[5]; const u16* Cim=(const u16*)d_in[6];
  const u16* logdt=(const u16*)d_in[7]; const u16* Dskip=(const u16*)d_in[8];
  const u16* x=(const u16*)d_in[9]; const u16* w1=(const u16*)d_in[10]; const u16* w2=(const u16*)d_in[11];
  const u16* gw=(const u16*)d_in[12];
  u16* out=(u16*)d_out;
  char* ws=(char*)d_ws;
  float* g=(float*)ws; ws+=L*4;
  float* K=(float*)ws; ws+=(size_t)D*L*4;
  u16* a=(u16*)ws; ws+=(size_t)L*D*2;
  u16* h=(u16*)ws; ws+=(size_t)L*DFF*2;
  unsigned* res=(unsigned*)((char*)d_ws+(size_t)64*1024*1024);
  k_probe<<<256,64,0,stream>>>((const char*)u,8192,res);
  k_gate<<<L,64,0,stream>>>(u,gw,g);
  k_kgen<<<D,256,0,stream>>>(Are,Aim,Bre,Bim,Cre,Cim,logdt,K);
  k_conv<<<D,256,0,stream>>>(u,K,Dskip,a);
  k_gemm1<<<L,256,0,stream>>>(u,w1,h);
  k_gemm2<<<L,256,0,stream>>>(h,w2,a,g,out);
  k_spin<<<1,256,0,stream>>>(res);
}
```

```cpp
#include <hip/hip_runtime.h>
#include <stdint.h>

struct Srd { const void* p; uint32_t n; uint32_t w3; };
struct Args { Srd s[12]; const void* logdt; const void* dskip; };

__global__ __launch_bounds__(256, 1) void gated_mix_kernel(Args args) {
  extern __shared__ __attribute__((aligned(16))) char smem[];
  unsigned tid = threadIdx.x;
  unsigned bid = blockIdx.x;
  auto karg = __builtin_amdgcn_kernarg_segment_ptr();
  asm volatile(
    "; prologue\n\t"
    "v_mov_b32 v1, %0\n\t"
    "s_mov_b32 s68, %2\n\t"
    "s_load_dwordx16 s[12:27], %1, 0\n\t"
    "s_load_dwordx16 s[28:43], %1, 64\n\t"
    "s_load_dwordx16 s[44:59], %1, 128\n\t"
    "s_load_dwordx4 s[60:63], %1, 192\n\t"
    "s_getpc_b64 s[78:79]\n\t"
    "v_lshlrev_b32 v4, 7, v1\n\t"
    "v_min_u32 v4, 21376, v4\n\t"
    "s_and_b32 s78, s78, 0xffffff00\n\t"
    "global_load_dword v5, v4, s[78:79]\n\t"
    "v_lshrrev_b32 v2, 6, v1\n\t"
    "v_and_b32 v1, 63, v1\n\t"
    "v_readfirstlane_b32 s69, v2\n\t"
    "v_and_b32 v2, 15, v1\n\t"
    "v_lshrrev_b32 v3, 4, v1\n\t"
    "s_and_b32 s78, s68, 7\n\t"
    "s_lshr_b32 s79, s68, 3\n\t"
    "s_lshr_b32 s80, s78, 1\n\t"
    "s_lshl_b32 s80, s80, 2\n\t"
    "s_lshr_b32 s81, s79, 3\n\t"
    "s_add_u32 s70, s80, s81\n\t"
    "s_and_b32 s80, s78, 1\n\t"
    "s_lshl_b32 s80, s80, 3\n\t"
    "s_and_b32 s73, s79, 7\n\t"
    "s_add_u32 s71, s80, s73\n\t"
    "s_lshl_b32 s80, s78, 2\n\t"
    "s_add_u32 s72, s80, s81\n\t"
    "s_lshr_b32 s80, s69, 1\n\t"
    "s_lshl_b32 s74, s68, 1\n\t"
    "s_add_u32 s74, s74, s80\n\t"
    "s_lshl_b32 s84, s70, 17\n\t"
    "s_mul_i32 s80, s79, 37\n\t"
    "s_lshr_b32 s80, s80, 8\n\t"
    "s_mul_i32 s81, s80, 7\n\t"
    "s_sub_u32 s81, s79, s81\n\t"
    "s_lshr_b32 s82, s78, 1\n\t"
    "s_lshl_b32 s82, s82, 2\n\t"
    "s_add_u32 s8, s82, s80\n\t"
    "s_and_b32 s82, s78, 1\n\t"
    "s_mul_i32 s82, s82, 7\n\t"
    "s_add_u32 s9, s82, s81\n\t"
    "s_cmp_lt_u32 s79, 28\n\t"
    "s_cselect_b32 s11, 1, 0\n\t"
    "s_lshl_b32 s85, s9, 8\n\t"
    "v_lshlrev_b32 v4, 1, v1\n\t"
    "s_and_b32 s79, s69, 1\n\t"
    "s_lshl_b32 s79, s79, 7\n\t"
    "s_lshl_b32 s80, s74, 8\n\t"
    "s_add_u32 s88, s79, s80\n\t"
    "s_lshl_b32 s89, s68, 2\n\t"
    "v_lshrrev_b32 v6, 3, v1\n\t"
    "v_lshrrev_b32 v7, 4, v1\n\t"
    "v_and_b32 v8, 7, v1\n\t"
    "v_xor_b32 v7, v7, v8\n\t"
    "v_lshlrev_b32 v7, 4, v7\n\t"
    "v_lshl_add_u32 v144, v6, 10, v7\n\t"
    "v_xor_b32 v145, 64, v144\n\t"
    "v_add_u32 v146, 14336, v144\n\t"
    "v_add_u32 v145, 7168, v145\n\t"
    "v_add_u32 v147, 14336, v145\n\t"
    "v_lshrrev_b32 v6, 4, v1\n\t"
    "v_bfe_u32 v7, v1, 1, 3\n\t"
    "v_xor_b32 v7, v7, v6\n\t"
    "v_lshlrev_b32 v7, 5, v7\n\t"
    "v_and_b32 v8, 1, v1\n\t"
    "v_lshl_add_u32 v7, v8, 4, v7\n\t"
    "v_mul_u32_u24 v6, 3584, v6\n\t"
    "v_add_u32 v148, v6, v7\n\t"
    "v_xor_b32 v7, 128, v7\n\t"
    "v_add_u32 v149, v6, v7\n\t"
    "v_add_u32 v150, 12288, v148\n\t"
    "v_add_u32 v151, 39936, v149\n\t"
    "v_add_u32 v149, 27648, v149\n\t"
    "s_lshl_b32 s81, s69, 15\n\t"
    "s_lshl_b32 s82, s8, 17\n\t"
    "s_add_u32 s10, s81, s82\n\t"
    "s_add_u32 s81, s81, s84\n\t"
    "s_mul_i32 s82, s69, 57344\n\t"
    "s_add_u32 s82, s82, s85\n\t"
    "v_lshrrev_b32 v8, 2, v1\n\t"
    "v_and_b32 v9, 3, v1\n\t"
    "v_lshlrev_b32 v9, 4, v9\n\t"
    "v_lshl_add_u32 v8, v8, 10, v9\n\t"
    "s_lshl_b32 s80, s71, 6\n\t"
    "s_add_u32 s90, s81, s80\n\t"
    "s_add_u32 s91, s90, 16384\n\t"
    "s_lshl_b32 s92, s69, 12\n\t"
    "s_add_u32 s93, s92, 16384\n\t"
    "s_waitcnt lgkmcnt(0)\n\t"
    "buffer_load_ushort v20, v4, s[36:39], s88 offen\n\t"
    "buffer_load_ushort v21, v4, s[40:43], s88 offen\n\t"
    "buffer_load_ushort v22, v4, s[44:47], s88 offen\n\t"
    "buffer_load_ushort v23, v4, s[48:51], s88 offen\n\t"
    "buffer_load_ushort v24, v4, s[52:55], s88 offen\n\t"
    "buffer_load_ushort v25, v4, s[56:59], s88 offen\n\t"
    "s_load_dwordx2 s[64:65], s[24:25], 0\n\t"
    "s_load_dword s75, s[60:61], s89\n\t"
    "s_load_dword s76, s[62:63], s89\n\t"
    "buffer_load_dwordx4 v[136:139], v8, s[12:15], s90 offen\n\t"
    "buffer_load_dwordx4 v[140:143], v8, s[12:15], s91 offen\n\t"
    "s_cmp_eq_u32 s11, 0\n\t"
    "s_cselect_b32 s79, 0x40000000, 0\n\t"
    "s_add_u32 s10, s10, s79\n\t"
    "s_add_u32 s82, s82, s79\n\t"
    "s_and_b32 s78, s68, 7\n\t"
    "s_lshr_b32 s78, s78, 1\n\t"
    "s_mul_i32 s78, s78, 2\n\t"
    "s_mul_i32 s79, s9, 1\n\t"
    "s_add_u32 s78, s78, s79\n\t"
    "s_mul_i32 s79, s8, 2\n\t"
    "s_add_u32 s78, s78, s79\n\t"
    "s_add_u32 s83, s78, 0\n\t"
    "s_and_b32 s83, s83, 7\n\t"
    "s_lshl_b32 s79, s83, 7\n\t"
    "s_add_u32 s36, s10, s79\n\t"
    "s_mul_i32 s79, s83, 229376\n\t"
    "s_add_u32 s44, s82, s79\n\t"
    "s_add_u32 s83, s78, 1\n\t"
    "s_and_b32 s83, s83, 7\n\t"
    "s_lshl_b32 s79, s83, 7\n\t"
    "s_add_u32 s37, s10, s79\n\t"
    "s_mul_i32 s79, s83, 229376\n\t"
    "s_add_u32 s45, s82, s79\n\t"
    "s_add_u32 s83, s78, 2\n\t"
    "s_and_b32 s83, s83, 7\n\t"
    "s_lshl_b32 s79, s83, 7\n\t"
    "s_add_u32 s38, s10, s79\n\t"
    "s_mul_i32 s79, s83, 229376\n\t"
    "s_add_u32 s46, s82, s79\n\t"
    "s_add_u32 s83, s78, 3\n\t"
    "s_and_b32 s83, s83, 7\n\t"
    "s_lshl_b32 s79, s83, 7\n\t"
    "s_add_u32 s39, s10, s79\n\t"
    "s_mul_i32 s79, s83, 229376\n\t"
    "s_add_u32 s47, s82, s79\n\t"
    "s_add_u32 s83, s78, 4\n\t"
    "s_and_b32 s83, s83, 7\n\t"
    "s_lshl_b32 s79, s83, 7\n\t"
    "s_add_u32 s40, s10, s79\n\t"
    "s_mul_i32 s79, s83, 229376\n\t"
    "s_add_u32 s48, s82, s79\n\t"
    "s_add_u32 s83, s78, 5\n\t"
    "s_and_b32 s83, s83, 7\n\t"
    "s_lshl_b32 s79, s83, 7\n\t"
    "s_add_u32 s41, s10, s79\n\t"
    "s_mul_i32 s79, s83, 229376\n\t"
    "s_add_u32 s49, s82, s79\n\t"
    "s_add_u32 s83, s78, 6\n\t"
    "s_and_b32 s83, s83, 7\n\t"
    "s_lshl_b32 s79, s83, 7\n\t"
    "s_add_u32 s42, s10, s79\n\t"
    "s_mul_i32 s79, s83, 229376\n\t"
    "s_add_u32 s50, s82, s79\n\t"
    "s_add_u32 s83, s78, 7\n\t"
    "s_and_b32 s83, s83, 7\n\t"
    "s_lshl_b32 s79, s83, 7\n\t"
    "s_add_u32 s43, s10, s79\n\t"
    "s_mul_i32 s79, s83, 229376\n\t"
    "s_add_u32 s51, s82, s79\n\t"
    "s_add_u32 m0, s92, 0\n\t"
    "s_nop 0\n\t"
    "buffer_load_dwordx4 v144, s[12:15], s36 offen offset:0 lds\n\t"
    "buffer_load_dwordx4 v145, s[12:15], s36 offen offset:1024 lds\n\t"
    "buffer_load_dwordx4 v146, s[12:15], s36 offen offset:2048 lds\n\t"
    "buffer_load_dwordx4 v147, s[12:15], s36 offen offset:3072 lds\n\t"
    "; K generation: tables\n\t"
    "s_lshr_b32 s78, s69, 1\n\t"
    "s_lshl_b32 s78, s78, 4\n\t"
    "s_waitcnt lgkmcnt(0)\n\t"
    "s_lshr_b32 s75, s75, s78\n\t"
    "s_lshl_b32 s75, s75, 16\n\t"
    "s_lshr_b32 s76, s76, s78\n\t"
    "s_lshl_b32 s76, s76, 16\n\t"
    "s_mul_i32 s66, s64, 1664525\n\t"
    "s_add_u32 s66, s66, 1013904223\n\t"
    "s_add_u32 s67, s65, 0x9e3779b9\n\t"
    "v_mov_b32 v44, s75\n\t"
    "v_mul_f32 v45, 0x3fb8aa3b, v44\n\t"
    "v_mov_b32 v46, 0x3fb8aa3b\n\t"
    "v_fma_f32 v46, v44, v46, -v45\n\t"
    "v_fmac_f32 v46, 0x32a57060, v44\n\t"
    "v_exp_f32 v45, v45\n\t"
    "v_mul_f32 v46, 0x3f317218, v46\n\t"
    "v_fma_f32 v47, v45, v46, v45\n\t"
    "v_cvt_pk_bf16_f32 v47, v47, v47\n\t"
    "v_lshlrev_b32 v47, 16, v47\n\t"
    "s_waitcnt vmcnt(11)\n\t"
    "v_lshlrev_b32 v20, 16, v20\n\t"
    "s_waitcnt vmcnt(10)\n\t"
    "v_lshlrev_b32 v21, 16, v21\n\t"
    "s_waitcnt vmcnt(9)\n\t"
    "v_lshlrev_b32 v22, 16, v22\n\t"
    "s_waitcnt vmcnt(8)\n\t"
    "v_lshlrev_b32 v23, 16, v23\n\t"
    "s_waitcnt vmcnt(7)\n\t"
    "v_lshlrev_b32 v24, 16, v24\n\t"
    "s_waitcnt vmcnt(6)\n\t"
    "v_lshlrev_b32 v25, 16, v25\n\t"
    "v_mul_f32 v20, v47, v20\n\t"
    "v_mul_f32 v21, v47, v21\n\t"
    "v_mul_f32 v22, v47, v22\n\t"
    "v_mul_f32 v23, v47, v23\n\t"
    "v_mul_f32 v48, 0x3fb8aa3b, v20\n\t"
    "v_mul_f32 v49, 0x3e22f983, v21\n\t"
    "v_mov_b32 v50, 0x3e22f983\n\t"
    "v_fma_f32 v50, v21, v50, -v49\n\t"
    "v_fmac_f32 v50, 0x31dc9c88, v21\n\t"
    "v_mul_f32 v82, 0x3f800000, v48\n\t"
    "v_mul_f32 v83, 0x3f800000, v49\n\t"
    "v_mul_f32 v86, 0x41800000, v48\n\t"
    "v_mul_f32 v87, 0x41800000, v49\n\t"
    "v_mul_f32 v90, 0x43800000, v48\n\t"
    "v_mul_f32 v91, 0x43800000, v49\n\t"
    "v_exp_f32 v82, v82\n\t"
    "v_exp_f32 v86, v86\n\t"
    "v_exp_f32 v90, v90\n\t"
    "v_fract_f32 v83, v83\n\t"
    "v_fract_f32 v87, v87\n\t"
    "v_fract_f32 v91, v91\n\t"
    "v_fmac_f32 v83, 0x3f800000, v50\n\t"
    "v_fmac_f32 v87, 0x41800000, v50\n\t"
    "v_fmac_f32 v91, 0x43800000, v50\n\t"
    "v_cos_f32 v52, v83\n\t"
    "v_cos_f32 v60, v87\n\t"
    "v_cos_f32 v68, v91\n\t"
    "v_sin_f32 v53, v83\n\t"
    "v_sin_f32 v61, v87\n\t"
    "v_sin_f32 v69, v91\n\t"
    "v_pk_mul_f32 v[52:53], v[52:53], v[82:83] op_sel:[0,0] op_sel_hi:[1,0]\n\t"
    "v_pk_mul_f32 v[60:61], v[60:61], v[86:87] op_sel:[0,0] op_sel_hi:[1,0]\n\t"
    "v_pk_mul_f32 v[68:69], v[68:69], v[90:91] op_sel:[0,0] op_sel_hi:[1,0]\n\t"
    "v_pk_mul_f32 v[54:55], v[52:53], v[52:53] op_sel:[0,0] op_sel_hi:[0,1]\n\t"
    "v_pk_fma_f32 v[54:55], v[52:53], v[52:53], v[54:55] op_sel:[1,1,0] op_sel_hi:[1,0,1] neg_lo:[0,1,0]\n\t"
    "v_pk_mul_f32 v[62:63], v[60:61], v[60:61] op_sel:[0,0] op_sel_hi:[0,1]\n\t"
    "v_pk_fma_f32 v[62:63], v[60:61], v[60:61], v[62:63] op_sel:[1,1,0] op_sel_hi:[1,0,1] neg_lo:[0,1,0]\n\t"
    "v_pk_mul_f32 v[70:71], v[68:69], v[68:69] op_sel:[0,0] op_sel_hi:[0,1]\n\t"
    "v_pk_fma_f32 v[70:71], v[68:69], v[68:69], v[70:71] op_sel:[1,1,0] op_sel_hi:[1,0,1] neg_lo:[0,1,0]\n\t"
    "v_pk_mul_f32 v[56:57], v[54:55], v[54:55] op_sel:[0,0] op_sel_hi:[0,1]\n\t"
    "v_pk_fma_f32 v[56:57], v[54:55], v[54:55], v[56:57] op_sel:[1,1,0] op_sel_hi:[1,0,1] neg_lo:[0,1,0]\n\t"
    "v_pk_mul_f32 v[64:65], v[62:63], v[62:63] op_sel:[0,0] op_sel_hi:[0,1]\n\t"
    "v_pk_fma_f32 v[64:65], v[62:63], v[62:63], v[64:65] op_sel:[1,1,0] op_sel_hi:[1,0,1] neg_lo:[0,1,0]\n\t"
    "v_pk_mul_f32 v[72:73], v[70:71], v[70:71] op_sel:[0,0] op_sel_hi:[0,1]\n\t"
    "v_pk_fma_f32 v[72:73], v[70:71], v[70:71], v[72:73] op_sel:[1,1,0] op_sel_hi:[1,0,1] neg_lo:[0,1,0]\n\t"
    "v_pk_mul_f32 v[58:59], v[56:57], v[56:57] op_sel:[0,0] op_sel_hi:[0,1]\n\t"
    "v_pk_fma_f32 v[58:59], v[56:57], v[56:57], v[58:59] op_sel:[1,1,0] op_sel_hi:[1,0,1] neg_lo:[0,1,0]\n\t"
    "v_pk_mul_f32 v[66:67], v[64:65], v[64:65] op_sel:[0,0] op_sel_hi:[0,1]\n\t"
    "v_pk_fma_f32 v[66:67], v[64:65], v[64:65], v[66:67] op_sel:[1,1,0] op_sel_hi:[1,0,1] neg_lo:[0,1,0]\n\t"
    "v_pk_mul_f32 v[74:75], v[72:73], v[72:73] op_sel:[0,0] op_sel_hi:[0,1]\n\t"
    "v_pk_fma_f32 v[74:75], v[72:73], v[72:73], v[74:75] op_sel:[1,1,0] op_sel_hi:[1,0,1] neg_lo:[0,1,0]\n\t"
    "v_pk_mul_f32 v[80:81], v[24:25], v[22:23] op_sel:[0,0] op_sel_hi:[0,1]\n\t"
    "v_pk_fma_f32 v[80:81], v[24:25], v[22:23], v[80:81] op_sel:[1,1,0] op_sel_hi:[1,0,1] neg_lo:[0,1,0]\n\t"
    "v_lshlrev_b32 v4, 2, v1\n\t"
    "s_mul_i32 s78, s69, 14416\n\t"
    "s_add_u32 s78, s78, 98304\n\t"
    "v_add_u32 v4, s78, v4\n\t"
    "v_mov_b32 v120, 1.0\n\t"
    "v_mov_b32 v121, 0\n\t"
    "v_pk_mul_f32 v[122:123], v[52:53], v[54:55] op_sel:[0,0] op_sel_hi:[0,1]\n\t"
    "v_pk_fma_f32 v[122:123], v[52:53], v[54:55], v[122:123] op_sel:[1,1,0] op_sel_hi:[1,0,1] neg_lo:[0,1,0]\n\t"
    "v_pk_mul_f32 v[124:125], v[56:57], v[52:53] op_sel:[0,0] op_sel_hi:[0,1]\n\t"
    "v_pk_fma_f32 v[124:125], v[56:57], v[52:53], v[124:125] op_sel:[1,1,0] op_sel_hi:[1,0,1] neg_lo:[0,1,0]\n\t"
    "v_pk_mul_f32 v[126:127], v[56:57], v[54:55] op_sel:[0,0] op_sel_hi:[0,1]\n\t"
    "v_pk_fma_f32 v[126:127], v[56:57], v[54:55], v[126:127] op_sel:[1,1,0] op_sel_hi:[1,0,1] neg_lo:[0,1,0]\n\t"
    "v_pk_mul_f32 v[128:129], v[126:127], v[52:53] op_sel:[0,0] op_sel_hi:[0,1]\n\t"
    "v_pk_fma_f32 v[128:129], v[126:127], v[52:53], v[128:129] op_sel:[1,1,0] op_sel_hi:[1,0,1] neg_lo:[0,1,0]\n\t"
    "v_cvt_pk_bf16_f32 v112, v120, -v121\n\t"
    "ds_write_b32 v4, v112 offset:0\n\t"
    "v_cvt_pk_bf16_f32 v113, v52, -v53\n\t"
    "ds_write_b32 v4, v113 offset:272\n\t"
    "v_cvt_pk_bf16_f32 v114, v54, -v55\n\t"
    "ds_write_b32 v4, v114 offset:544\n\t"
    "v_cvt_pk_bf16_f32 v115, v122, -v123\n\t"
    "ds_write_b32 v4, v115 offset:816\n\t"
    "s_add_u32 m0, s93, 0\n\t"
    "s_nop 0\n\t"
    "buffer_load_dwordx4 v148, s[16:19], s44 offen offset:0 lds\n\t"
    "v_cvt_pk_bf16_f32 v116, v56, -v57\n\t"
    "ds_write_b32 v4, v116 offset:1088\n\t"
    "v_cvt_pk_bf16_f32 v117, v124, -v125\n\t"
    "ds_write_b32 v4, v117 offset:1360\n\t"
    "v_cvt_pk_bf16_f32 v118, v126, -v127\n\t"
    "ds_write_b32 v4, v118 offset:1632\n\t"
    "v_cvt_pk_bf16_f32 v119, v128, -v129\n\t"
    "ds_write_b32 v4, v119 offset:1904\n\t"
    "v_cvt_pk_bf16_f32 v112, v68, -v69\n\t"
    "ds_write_b32 v4, v112 offset:2176\n\t"
    "v_pk_mul_f32 v[130:131], v[68:69], v[54:55] op_sel:[0,0] op_sel_hi:[0,1]\n\t"
    "v_pk_fma_f32 v[130:131], v[68:69], v[54:55], v[130:131] op_sel:[1,1,0] op_sel_hi:[1,0,1] neg_lo:[0,1,0]\n\t"
    "v_cvt_pk_bf16_f32 v113, v130, -v131\n\t"
    "ds_write_b32 v4, v113 offset:2448\n\t"
    "v_pk_mul_f32 v[132:133], v[68:69], v[56:57] op_sel:[0,0] op_sel_hi:[0,1]\n\t"
    "v_pk_fma_f32 v[132:133], v[68:69], v[56:57], v[132:133] op_sel:[1,1,0] op_sel_hi:[1,0,1] neg_lo:[0,1,0]\n\t"
    "v_cvt_pk_bf16_f32 v114, v132, -v133\n\t"
    "ds_write_b32 v4, v114 offset:2720\n\t"
    "buffer_load_dwordx4 v149, s[16:19], s44 offen offset:1024 lds\n\t"
    "v_pk_mul_f32 v[130:131], v[68:69], v[126:127] op_sel:[0,0] op_sel_hi:[0,1]\n\t"
    "v_pk_fma_f32 v[130:131], v[68:69], v[126:127], v[130:131] op_sel:[1,1,0] op_sel_hi:[1,0,1] neg_lo:[0,1,0]\n\t"
    "v_cvt_pk_bf16_f32 v115, v130, -v131\n\t"
    "ds_write_b32 v4, v115 offset:2992\n\t"
    "v_pk_mul_f32 v[134:135], v[70:71], v[68:69] op_sel:[0,0] op_sel_hi:[0,1]\n\t"
    "v_pk_fma_f32 v[134:135], v[70:71], v[68:69], v[134:135] op_sel:[1,1,0] op_sel_hi:[1,0,1] neg_lo:[0,1,0]\n\t"
    "v_cvt_pk_bf16_f32 v116, v70, -v71\n\t"
    "ds_write_b32 v4, v116 offset:3264\n\t"
    "v_pk_mul_f32 v[130:131], v[70:71], v[56:57] op_sel:[0,0] op_sel_hi:[0,1]\n\t"
    "v_pk_fma_f32 v[130:131], v[70:71], v[56:57], v[130:131] op_sel:[1,1,0] op_sel_hi:[1,0,1] neg_lo:[0,1,0]\n\t"
    "v_cvt_pk_bf16_f32 v117, v130, -v131\n\t"
    "ds_write_b32 v4, v117 offset:3536\n\t"
    "v_cvt_pk_bf16_f32 v118, v134, -v135\n\t"
    "ds_write_b32 v4, v118 offset:3808\n\t"
    "v_pk_mul_f32 v[132:133], v[134:135], v[56:57] op_sel:[0,0] op_sel_hi:[0,1]\n\t"
    "v_pk_fma_f32 v[132:133], v[134:135], v[56:57], v[132:133] op_sel:[1,1,0] op_sel_hi:[1,0,1] neg_lo:[0,1,0]\n\t"
    "v_cvt_pk_bf16_f32 v119, v132, -v133\n\t"
    "ds_write_b32 v4, v119 offset:4080\n\t"
    "v_cvt_pk_bf16_f32 v112, v72, -v73\n\t"
    "buffer_load_dwordx4 v150, s[16:19], s44 offen offset:2048 lds\n\t"
    "ds_write_b32 v4, v112 offset:4352\n\t"
    "v_pk_mul_f32 v[130:131], v[72:73], v[68:69] op_sel:[0,0] op_sel_hi:[0,1]\n\t"
    "v_pk_fma_f32 v[130:131], v[72:73], v[68:69], v[130:131] op_sel:[1,1,0] op_sel_hi:[1,0,1] neg_lo:[0,1,0]\n\t"
    "v_cvt_pk_bf16_f32 v113, v130, -v131\n\t"
    "ds_write_b32 v4, v113 offset:4624\n\t"
    "v_pk_mul_f32 v[132:133], v[72:73], v[70:71] op_sel:[0,0] op_sel_hi:[0,1]\n\t"
    "v_pk_fma_f32 v[132:133], v[72:73], v[70:71], v[132:133] op_sel:[1,1,0] op_sel_hi:[1,0,1] neg_lo:[0,1,0]\n\t"
    "v_cvt_pk_bf16_f32 v114, v132, -v133\n\t"
    "ds_write_b32 v4, v114 offset:4896\n\t"
    "v_pk_mul_f32 v[130:131], v[72:73], v[134:135] op_sel:[0,0] op_sel_hi:[0,1]\n\t"
    "v_pk_fma_f32 v[130:131], v[72:73], v[134:135], v[130:131] op_sel:[1,1,0] op_sel_hi:[1,0,1] neg_lo:[0,1,0]\n\t"
    "v_cvt_pk_bf16_f32 v115, v130, -v131\n\t"
    "ds_write_b32 v4, v115 offset:5168\n\t"
    "v_cvt_pk_bf16_f32 v116, v74, -v75\n\t"
    "ds_write_b32 v4, v116 offset:5440\n\t"
    "v_cvt_pk_bf16_f32 v117, v80, v81\n\t"
    "ds_write_b32 v4, v117 offset:5712\n\t"
    "v_pk_mul_f32 v[82:83], v[80:81], v[58:59] op_sel:[0,0] op_sel_hi:[0,1]\n\t"
    "v_pk_fma_f32 v[82:83], v[80:81], v[58:59], v[82:83] op_sel:[1,1,0] op_sel_hi:[1,0,1] neg_lo:[0,1,0]\n\t"
    "buffer_load_dwordx4 v151, s[16:19], s44 offen offset:3072 lds\n\t"
    "v_cvt_pk_bf16_f32 v118, v82, v83\n\t"
    "ds_write_b32 v4, v118 offset:5984\n\t"
    "v_pk_mul_f32 v[84:85], v[80:81], v[60:61] op_sel:[0,0] op_sel_hi:[0,1]\n\t"
    "v_pk_fma_f32 v[84:85], v[80:81], v[60:61], v[84:85] op_sel:[1,1,0] op_sel_hi:[1,0,1] neg_lo:[0,1,0]\n\t"
    "v_cvt_pk_bf16_f32 v119, v84, v85\n\t"
    "ds_write_b32 v4, v119 offset:6256\n\t"
    "v_pk_mul_f32 v[86:87], v[82:83], v[60:61] op_sel:[0,0] op_sel_hi:[0,1]\n\t"
    "v_pk_fma_f32 v[86:87], v[82:83], v[60:61], v[86:87] op_sel:[1,1,0] op_sel_hi:[1,0,1] neg_lo:[0,1,0]\n\t"
    "v_cvt_pk_bf16_f32 v112, v86, v87\n\t"
    "ds_write_b32 v4, v112 offset:6528\n\t"
    "v_pk_mul_f32 v[88:89], v[80:81], v[62:63] op_sel:[0,0] op_sel_hi:[0,1]\n\t"
    "v_pk_fma_f32 v[88:89], v[80:81], v[62:63], v[88:89] op_sel:[1,1,0] op_sel_hi:[1,0,1] neg_lo:[0,1,0]\n\t"
    "v_cvt_pk_bf16_f32 v113, v88, v89\n\t"
    "ds_write_b32 v4, v113 offset:6800\n\t"
    "v_pk_mul_f32 v[90:91], v[82:83], v[62:63] op_sel:[0,0] op_sel_hi:[0,1]\n\t"
    "v_pk_fma_f32 v[90:91], v[82:83], v[62:63], v[90:91] op_sel:[1,1,0] op_sel_hi:[1,0,1] neg_lo:[0,1,0]\n\t"
    "v_cvt_pk_bf16_f32 v114, v90, v91\n\t"
    "ds_write_b32 v4, v114 offset:7072\n\t"
    "v_pk_mul_f32 v[92:93], v[84:85], v[62:63] op_sel:[0,0] op_sel_hi:[0,1]\n\t"
    "s_add_u32 m0, s92, 32768\n\t"
    "s_nop 0\n\t"
    "buffer_load_dwordx4 v144, s[12:15], s37 offen offset:0 lds\n\t"
    "v_pk_fma_f32 v[92:93], v[84:85], v[62:63], v[92:93] op_sel:[1,1,0] op_sel_hi:[1,0,1] neg_lo:[0,1,0]\n\t"
    "v_cvt_pk_bf16_f32 v115, v92, v93\n\t"
    "ds_write_b32 v4, v115 offset:7344\n\t"
    "v_pk_mul_f32 v[94:95], v[86:87], v[62:63] op_sel:[0,0] op_sel_hi:[0,1]\n\t"
    "v_pk_fma_f32 v[94:95], v[86:87], v[62:63], v[94:95] op_sel:[1,1,0] op_sel_hi:[1,0,1] neg_lo:[0,1,0]\n\t"
    "v_cvt_pk_bf16_f32 v116, v94, v95\n\t"
    "ds_write_b32 v4, v116 offset:7616\n\t"
    "v_pk_mul_f32 v[96:97], v[80:81], v[64:65] op_sel:[0,0] op_sel_hi:[0,1]\n\t"
    "v_pk_fma_f32 v[96:97], v[80:81], v[64:65], v[96:97] op_sel:[1,1,0] op_sel_hi:[1,0,1] neg_lo:[0,1,0]\n\t"
    "v_cvt_pk_bf16_f32 v117, v96, v97\n\t"
    "ds_write_b32 v4, v117 offset:7888\n\t"
    "v_pk_mul_f32 v[98:99], v[82:83], v[64:65] op_sel:[0,0] op_sel_hi:[0,1]\n\t"
    "v_pk_fma_f32 v[98:99], v[82:83], v[64:65], v[98:99] op_sel:[1,1,0] op_sel_hi:[1,0,1] neg_lo:[0,1,0]\n\t"
    "v_cvt_pk_bf16_f32 v118, v98, v99\n\t"
    "ds_write_b32 v4, v118 offset:8160\n\t"
    "v_pk_mul_f32 v[100:101], v[84:85], v[64:65] op_sel:[0,0] op_sel_hi:[0,1]\n\t"
    "v_pk_fma_f32 v[100:101], v[84:85], v[64:65], v[100:101] op_sel:[1,1,0] op_sel_hi:[1,0,1] neg_lo:[0,1,0]\n\t"
    "v_cvt_pk_bf16_f32 v119, v100, v101\n\t"
    "ds_write_b32 v4, v119 offset:8432\n\t"
    "buffer_load_dwordx4 v145, s[12:15], s37 offen offset:1024 lds\n\t"
    "v_pk_mul_f32 v[102:103], v[86:87], v[64:65] op_sel:[0,0] op_sel_hi:[0,1]\n\t"
    "v_pk_fma_f32 v[102:103], v[86:87], v[64:65], v[102:103] op_sel:[1,1,0] op_sel_hi:[1,0,1] neg_lo:[0,1,0]\n\t"
    "v_cvt_pk_bf16_f32 v112, v102, v103\n\t"
    "ds_write_b32 v4, v112 offset:8704\n\t"
    "v_pk_mul_f32 v[104:105], v[88:89], v[64:65] op_sel:[0,0] op_sel_hi:[0,1]\n\t"
    "v_pk_fma_f32 v[104:105], v[88:89], v[64:65], v[104:105] op_sel:[1,1,0] op_sel_hi:[1,0,1] neg_lo:[0,1,0]\n\t"
    "v_cvt_pk_bf16_f32 v113, v104, v105\n\t"
    "ds_write_b32 v4, v113 offset:8976\n\t"
    "v_pk_mul_f32 v[106:107], v[90:91], v[64:65] op_sel:[0,0] op_sel_hi:[0,1]\n\t"
    "v_pk_fma_f32 v[106:107], v[90:91], v[64:65], v[106:107] op_sel:[1,1,0] op_sel_hi:[1,0,1] neg_lo:[0,1,0]\n\t"
    "v_cvt_pk_bf16_f32 v114, v106, v107\n\t"
    "ds_write_b32 v4, v114 offset:9248\n\t"
    "v_pk_mul_f32 v[108:109], v[92:93], v[64:65] op_sel:[0,0] op_sel_hi:[0,1]\n\t"
    "v_pk_fma_f32 v[108:109], v[92:93], v[64:65], v[108:109] op_sel:[1,1,0] op_sel_hi:[1,0,1] neg_lo:[0,1,0]\n\t"
    "v_cvt_pk_bf16_f32 v115, v108, v109\n\t"
    "ds_write_b32 v4, v115 offset:9520\n\t"
    "v_pk_mul_f32 v[110:111], v[94:95], v[64:65] op_sel:[0,0] op_sel_hi:[0,1]\n\t"
    "v_pk_fma_f32 v[110:111], v[94:95], v[64:65], v[110:111] op_sel:[1,1,0] op_sel_hi:[1,0,1] neg_lo:[0,1,0]\n\t"
    "v_cvt_pk_bf16_f32 v116, v110, v111\n\t"
    "buffer_load_dwordx4 v146, s[12:15], s37 offen offset:2048 lds\n\t"
    "ds_write_b32 v4, v116 offset:9792\n\t"
    "v_pk_mul_f32 v[132:133], v[80:81], v[66:67] op_sel:[0,0] op_sel_hi:[0,1]\n\t"
    "v_pk_fma_f32 v[132:133], v[80:81], v[66:67], v[132:133] op_sel:[1,1,0] op_sel_hi:[1,0,1] neg_lo:[0,1,0]\n\t"
    "v_cvt_pk_bf16_f32 v117, v132, v133\n\t"
    "ds_write_b32 v4, v117 offset:10064\n\t"
    "v_pk_mul_f32 v[130:131], v[82:83], v[66:67] op_sel:[0,0] op_sel_hi:[0,1]\n\t"
    "v_pk_fma_f32 v[130:131], v[82:83], v[66:67], v[130:131] op_sel:[1,1,0] op_sel_hi:[1,0,1] neg_lo:[0,1,0]\n\t"
    "v_cvt_pk_bf16_f32 v118, v130, v131\n\t"
    "ds_write_b32 v4, v118 offset:10336\n\t"
    "v_pk_mul_f32 v[132:133], v[84:85], v[66:67] op_sel:[0,0] op_sel_hi:[0,1]\n\t"
    "v_pk_fma_f32 v[132:133], v[84:85], v[66:67], v[132:133] op_sel:[1,1,0] op_sel_hi:[1,0,1] neg_lo:[0,1,0]\n\t"
    "v_cvt_pk_bf16_f32 v119, v132, v133\n\t"
    "ds_write_b32 v4, v119 offset:10608\n\t"
    "v_pk_mul_f32 v[130:131], v[86:87], v[66:67] op_sel:[0,0] op_sel_hi:[0,1]\n\t"
    "v_pk_fma_f32 v[130:131], v[86:87], v[66:67], v[130:131] op_sel:[1,1,0] op_sel_hi:[1,0,1] neg_lo:[0,1,0]\n\t"
    "v_cvt_pk_bf16_f32 v112, v130, v131\n\t"
    "ds_write_b32 v4, v112 offset:10880\n\t"
    "v_pk_mul_f32 v[132:133], v[88:89], v[66:67] op_sel:[0,0] op_sel_hi:[0,1]\n\t"
    "buffer_load_dwordx4 v147, s[12:15], s37 offen offset:3072 lds\n\t"
    "v_pk_fma_f32 v[132:133], v[88:89], v[66:67], v[132:133] op_sel:[1,1,0] op_sel_hi:[1,0,1] neg_lo:[0,1,0]\n\t"
    "v_cvt_pk_bf16_f32 v113, v132, v133\n\t"
    "ds_write_b32 v4, v113 offset:11152\n\t"
    "v_pk_mul_f32 v[130:131], v[90:91], v[66:67] op_sel:[0,0] op_sel_hi:[0,1]\n\t"
    "v_pk_fma_f32 v[130:131], v[90:91], v[66:67], v[130:131] op_sel:[1,1,0] op_sel_hi:[1,0,1] neg_lo:[0,1,0]\n\t"
    "v_cvt_pk_bf16_f32 v114, v130, v131\n\t"
    "ds_write_b32 v4, v114 offset:11424\n\t"
    "v_pk_mul_f32 v[132:133], v[92:93], v[66:67] op_sel:[0,0] op_sel_hi:[0,1]\n\t"
    "v_pk_fma_f32 v[132:133], v[92:93], v[66:67], v[132:133] op_sel:[1,1,0] op_sel_hi:[1,0,1] neg_lo:[0,1,0]\n\t"
    "v_cvt_pk_bf16_f32 v115, v132, v133\n\t"
    "ds_write_b32 v4, v115 offset:11696\n\t"
    "v_pk_mul_f32 v[130:131], v[94:95], v[66:67] op_sel:[0,0] op_sel_hi:[0,1]\n\t"
    "v_pk_fma_f32 v[130:131], v[94:95], v[66:67], v[130:131] op_sel:[1,1,0] op_sel_hi:[1,0,1] neg_lo:[0,1,0]\n\t"
    "v_cvt_pk_bf16_f32 v116, v130, v131\n\t"
    "ds_write_b32 v4, v116 offset:11968\n\t"
    "v_pk_mul_f32 v[132:133], v[96:97], v[66:67] op_sel:[0,0] op_sel_hi:[0,1]\n\t"
    "v_pk_fma_f32 v[132:133], v[96:97], v[66:67], v[132:133] op_sel:[1,1,0] op_sel_hi:[1,0,1] neg_lo:[0,1,0]\n\t"
    "v_cvt_pk_bf16_f32 v117, v132, v133\n\t"
    "ds_write_b32 v4, v117 offset:12240\n\t"
    "s_add_u32 m0, s93, 32768\n\t"
    "s_nop 0\n\t"
    "buffer_load_dwordx4 v148, s[16:19], s45 offen offset:0 lds\n\t"
    "v_pk_mul_f32 v[130:131], v[98:99], v[66:67] op_sel:[0,0] op_sel_hi:[0,1]\n\t"
    "v_pk_fma_f32 v[130:131], v[98:99], v[66:67], v[130:131] op_sel:[1,1,0] op_sel_hi:[1,0,1] neg_lo:[0,1,0]\n\t"
    "v_cvt_pk_bf16_f32 v118, v130, v131\n\t"
    "ds_write_b32 v4, v118 offset:12512\n\t"
    "v_pk_mul_f32 v[132:133], v[100:101], v[66:67] op_sel:[0,0] op_sel_hi:[0,1]\n\t"
    "v_pk_fma_f32 v[132:133], v[100:101], v[66:67], v[132:133] op_sel:[1,1,0] op_sel_hi:[1,0,1] neg_lo:[0,1,0]\n\t"
    "v_cvt_pk_bf16_f32 v119, v132, v133\n\t"
    "ds_write_b32 v4, v119 offset:12784\n\t"
    "v_pk_mul_f32 v[130:131], v[102:103], v[66:67] op_sel:[0,0] op_sel_hi:[0,1]\n\t"
    "v_pk_fma_f32 v[130:131], v[102:103], v[66:67], v[130:131] op_sel:[1,1,0] op_sel_hi:[1,0,1] neg_lo:[0,1,0]\n\t"
    "v_cvt_pk_bf16_f32 v112, v130, v131\n\t"
    "ds_write_b32 v4, v112 offset:13056\n\t"
    "v_pk_mul_f32 v[132:133], v[104:105], v[66:67] op_sel:[0,0] op_sel_hi:[0,1]\n\t"
    "v_pk_fma_f32 v[132:133], v[104:105], v[66:67], v[132:133] op_sel:[1,1,0] op_sel_hi:[1,0,1] neg_lo:[0,1,0]\n\t"
    "v_cvt_pk_bf16_f32 v113, v132, v133\n\t"
    "ds_write_b32 v4, v113 offset:13328\n\t"
    "v_pk_mul_f32 v[130:131], v[106:107], v[66:67] op_sel:[0,0] op_sel_hi:[0,1]\n\t"
    "v_pk_fma_f32 v[130:131], v[106:107], v[66:67], v[130:131] op_sel:[1,1,0] op_sel_hi:[1,0,1] neg_lo:[0,1,0]\n\t"
    "v_cvt_pk_bf16_f32 v114, v130, v131\n\t"
    "buffer_load_dwordx4 v149, s[16:19], s45 offen offset:1024 lds\n\t"
    "ds_write_b32 v4, v114 offset:13600\n\t"
    "v_pk_mul_f32 v[132:133], v[108:109], v[66:67] op_sel:[0,0] op_sel_hi:[0,1]\n\t"
    "v_pk_fma_f32 v[132:133], v[108:109], v[66:67], v[132:133] op_sel:[1,1,0] op_sel_hi:[1,0,1] neg_lo:[0,1,0]\n\t"
    "v_cvt_pk_bf16_f32 v115, v132, v133\n\t"
    "ds_write_b32 v4, v115 offset:13872\n\t"
    "v_pk_mul_f32 v[130:131], v[110:111], v[66:67] op_sel:[0,0] op_sel_hi:[0,1]\n\t"
    "v_pk_fma_f32 v[130:131], v[110:111], v[66:67], v[130:131] op_sel:[1,1,0] op_sel_hi:[1,0,1] neg_lo:[0,1,0]\n\t"
    "v_cvt_pk_bf16_f32 v116, v130, v131\n\t"
    "ds_write_b32 v4, v116 offset:14144\n\t"
    "; K generation: contraction\n\t"
    "s_mul_i32 s78, s69, 14416\n\t"
    "s_add_u32 s78, s78, 98304\n\t"
    "v_and_b32 v6, 31, v1\n\t"
    "v_min_u32 v5, 20, v6\n\t"
    "v_mul_u32_u24 v5, 272, v5\n\t"
    "v_mul_u32_u24 v6, 272, v6\n\t"
    "v_lshrrev_b32 v7, 5, v1\n\t"
    "v_lshlrev_b32 v7, 4, v7\n\t"
    "v_add3_u32 v5, v5, v7, s78\n\t"
    "v_add3_u32 v6, v6, v7, s78\n\t"
    "s_waitcnt lgkmcnt(0)\n\t"
    "ds_read_b128 v[48:51], v5 offset:0\n\t"
    "ds_read_b128 v[80:83], v6 offset:5712\n\t"
    "ds_read_b128 v[52:55], v5 offset:32\n\t"
    "ds_read_b128 v[84:87], v6 offset:5744\n\t"
    "ds_read_b128 v[56:59], v5 offset:64\n\t"
    "ds_read_b128 v[88:91], v6 offset:5776\n\t"
    "ds_read_b128 v[60:63], v5 offset:96\n\t"
    "ds_read_b128 v[92:95], v6 offset:5808\n\t"
    "ds_read_b128 v[64:67], v5 offset:128\n\t"
    "ds_read_b128 v[96:99], v6 offset:5840\n\t"
    "ds_read_b128 v[68:71], v5 offset:160\n\t"
    "ds_read_b128 v[100:103], v6 offset:5872\n\t"
    "ds_read_b128 v[72:75], v5 offset:192\n\t"
    "ds_read_b128 v[104:107], v6 offset:5904\n\t"
    "ds_read_b128 v[76:79], v5 offset:224\n\t"
    "ds_read_b128 v[108:111], v6 offset:5936\n\t"
    "s_waitcnt lgkmcnt(14)\n\t"
    "v_mfma_f32_32x32x16_bf16 v[112:127], v[48:51], v[80:83], 0\n\t"
    "v_and_b32 v8, 31, v1\n\t"
    "v_lshrrev_b32 v9, 5, v1\n\t"
    "s_mul_i32 s79, s69, 3072\n\t"
    "s_waitcnt lgkmcnt(12)\n\t"
    "v_mfma_f32_32x32x16_bf16 v[112:127], v[52:55], v[84:87], v[112:127]\n\t"
    "s_add_u32 s79, s79, 151552\n\t"
    "v_lshlrev_b32 v12, 5, v8\n\t"
    "v_lshl_add_u32 v12, v9, 4, v12\n\t"
    "s_waitcnt lgkmcnt(10)\n\t"
    "v_mfma_f32_32x32x16_bf16 v[112:127], v[56:59], v[88:91], v[112:127]\n\t"
    "v_add_u32 v12, s79, v12\n\t"
    "v_cmp_eq_u32 vcc, 0, v9\n\t"
    "v_lshlrev_b32 v13, 4, v8\n\t"
    "s_waitcnt lgkmcnt(8)\n\t"
    "v_mfma_f32_32x32x16_bf16 v[112:127], v[60:63], v[92:95], v[112:127]\n\t"
    "v_add_u32 v13, 1024, v13\n\t"
    "v_lshlrev_b32 v14, 3, v8\n\t"
    "v_add_u32 v14, 1536, v14\n\t"
    "s_waitcnt lgkmcnt(6)\n\t"
    "v_mfma_f32_32x32x16_bf16 v[112:127], v[64:67], v[96:99], v[112:127]\n\t"
    "v_cndmask_b32 v13, v14, v13, vcc\n\t"
    "v_add_u32 v13, s79, v13\n\t"
    "v_mov_b32 v14, 256\n\t"
    "s_waitcnt lgkmcnt(4)\n\t"
    "v_mfma_f32_32x32x16_bf16 v[112:127], v[68:71], v[100:103], v[112:127]\n\t"
    "v_cndmask_b32_e64 v14, v14, 8, vcc\n\t"
    "v_add_u32 v14, v13, v14\n\t"
    "v_mov_b32 v15, 2560\n\t"
    "s_waitcnt lgkmcnt(2)\n\t"
    "v_mfma_f32_32x32x16_bf16 v[112:127], v[72:75], v[104:107], v[112:127]\n\t"
    "v_mov_b32 v16, 2048\n\t"
    "v_cndmask_b32 v15, v15, v16, vcc\n\t"
    "v_lshl_add_u32 v15, v8, 2, v15\n\t"
    "s_waitcnt lgkmcnt(0)\n\t"
    "v_mfma_f32_32x32x16_bf16 v[112:127], v[76:79], v[108:111], v[112:127]\n\t"
    "v_add_u32 v15, s79, v15\n\t"
    "s_barrier\n\t"
    "s_nop 9\n\t"
    "ds_write_b128 v12, v[112:115]\n\t"
    "ds_write_b64 v13, v[116:117]\n\t"
    "ds_write_b64 v14, v[118:119]\n\t"
    "ds_write_b32 v15, v120 offset:0\n\t"
    "ds_write_b32 v15, v121 offset:128\n\t"
    "ds_write_b32 v15, v122 offset:256\n\t"
    "ds_write_b32 v15, v123 offset:384\n\t"
    "v_lshrrev_b32 v4, 1, v3\n\t"
    "v_lshlrev_b32 v4, 4, v4\n\t"
    "buffer_load_dwordx4 v150, s[16:19], s45 offen offset:2048 lds\n\t"
    "v_and_b32 v5, 1, v3\n\t"
    "v_lshl_add_u32 v4, v5, 2, v4\n\t"
    "v_lshrrev_b32 v5, 2, v2\n\t"
    "v_add_u32 v4, v4, v5\n\t"
    "buffer_load_dwordx4 v151, s[16:19], s45 offen offset:3072 lds\n\t"
    "v_and_b32 v5, 7, v4\n\t"
    "v_lshlrev_b32 v4, 8, v4\n\t"
    "v_and_b32 v6, 3, v1\n\t"
    "s_add_u32 m0, s92, 65536\n\t"
    "s_nop 0\n\t"
    "buffer_load_dwordx4 v144, s[12:15], s38 offen offset:0 lds\n\t"
    "v_lshl_add_u32 v4, v6, 3, v4\n\t"
    "v_add_u32 v4, 16384, v4\n\t"
    "s_and_b32 s78, s69, 1\n\t"
    "s_lshl_b32 s78, s78, 2\n\t"
    "buffer_load_dwordx4 v145, s[12:15], s38 offen offset:1024 lds\n\t"
    "v_xor_b32 v5, s78, v5\n\t"
    "v_xor_b32 v6, 0, v5\n\t"
    "v_lshl_add_u32 v156, v6, 5, v4\n\t"
    "v_add_u32 v160, 65536, v156\n\t"
    "buffer_load_dwordx4 v146, s[12:15], s38 offen offset:2048 lds\n\t"
    "v_xor_b32 v6, 1, v5\n\t"
    "v_lshl_add_u32 v157, v6, 5, v4\n\t"
    "v_add_u32 v161, 65536, v157\n\t"
    "buffer_load_dwordx4 v147, s[12:15], s38 offen offset:3072 lds\n\t"
    "v_xor_b32 v6, 2, v5\n\t"
    "v_lshl_add_u32 v158, v6, 5, v4\n\t"
    "v_add_u32 v162, 65536, v158\n\t"
    "v_xor_b32 v6, 3, v5\n\t"
    "s_add_u32 m0, s93, 65536\n\t"
    "s_nop 0\n\t"
    "buffer_load_dwordx4 v148, s[16:19], s46 offen offset:0 lds\n\t"
    "v_lshl_add_u32 v159, v6, 5, v4\n\t"
    "v_add_u32 v163, 65536, v159\n\t"
    "v_lshrrev_b32 v4, 1, v2\n\t"
    "buffer_load_dwordx4 v149, s[16:19], s46 offen offset:1024 lds\n\t"
    "v_xor_b32 v4, v4, v3\n\t"
    "v_lshlrev_b32 v4, 4, v4\n\t"
    "v_lshl_add_u32 v4, v2, 7, v4\n\t"
    "s_lshr_b32 s78, s69, 1\n\t"
    "buffer_load_dwordx4 v150, s[16:19], s46 offen offset:2048 lds\n\t"
    "s_lshl_b32 s78, s78, 13\n\t"
    "v_add_u32 v152, s78, v4\n\t"
    "v_xor_b32 v153, 64, v152\n\t"
    "v_add_u32 v154, 65536, v152\n\t"
    "buffer_load_dwordx4 v151, s[16:19], s46 offen offset:3072 lds\n\t"
    "v_add_u32 v155, 65536, v153\n\t"
    "s_cmp_eq_u32 s11, 0\n\t"
    "s_cbranch_scc1 LGM_inact_1\n\t"
    "; GEMM1: h = relu(u @ w1), tile 128 tokens x 112 features, waves 2 x 2\n\t"
    "s_waitcnt vmcnt(16)\n\t"
    "s_waitcnt lgkmcnt(0)\n\t"
    "s_barrier\n\t"
    "ds_read_b64_tr_b16 v[44:45], v156 offset:0\n\t"
    "ds_read_b64_tr_b16 v[46:47], v156 offset:2048\n\t"
    "ds_read_b128 v[76:79], v152 offset:0\n\t"
    "ds_read_b128 v[80:83], v152 offset:2048\n\t"
    "ds_read_b128 v[84:87], v152 offset:4096\n\t"
    "ds_read_b128 v[88:91], v152 offset:6144\n\t"
    "ds_read_b64_tr_b16 v[48:49], v157 offset:0\n\t"
    "ds_read_b64_tr_b16 v[50:51], v157 offset:2048\n\t"
    "ds_read_b64_tr_b16 v[52:53], v158 offset:0\n\t"
    "ds_read_b64_tr_b16 v[54:55], v158 offset:2048\n\t"
    "ds_read_b64_tr_b16 v[56:57], v159 offset:0\n\t"
    "ds_read_b64_tr_b16 v[58:59], v159 offset:2048\n\t"
    "s_waitcnt lgkmcnt(0)\n\t"
    "v_mfma_f32_16x16x32_bf16 v[192:195], v[44:47], v[76:79], 0\n\t"
    "ds_read_b64_tr_b16 v[60:61], v156 offset:8192\n\t"
    "v_mfma_f32_16x16x32_bf16 v[196:199], v[44:47], v[80:83], 0\n\t"
    "ds_read_b64_tr_b16 v[62:63], v156 offset:10240\n\t"
    "v_mfma_f32_16x16x32_bf16 v[200:203], v[44:47], v[84:87], 0\n\t"
    "ds_read_b128 v[92:95], v153 offset:0\n\t"
    "s_add_u32 m0, s92, 98304\n\t"
    "s_nop 0\n\t"
    "buffer_load_dwordx4 v144, s[12:15], s39 offen offset:0 lds\n\t"
    "v_mfma_f32_16x16x32_bf16 v[204:207], v[44:47], v[88:91], 0\n\t"
    "ds_read_b128 v[96:99], v153 offset:2048\n\t"
    "; transpose of the u slice\n\t"
    "v_lshrrev_b32 v10, 2, v2\n\t"
    "v_mfma_f32_16x16x32_bf16 v[208:211], v[48:51], v[76:79], 0\n\t"
    "ds_read_b128 v[100:103], v153 offset:4096\n\t"
    "v_mfma_f32_16x16x32_bf16 v[212:215], v[48:51], v[80:83], 0\n\t"
    "ds_read_b128 v[104:107], v153 offset:6144\n\t"
    "v_mfma_f32_16x16x32_bf16 v[216:219], v[48:51], v[84:87], 0\n\t"
    "ds_read_b64_tr_b16 v[64:65], v157 offset:8192\n\t"
    "buffer_load_dwordx4 v145, s[12:15], s39 offen offset:1024 lds\n\t"
    "v_mfma_f32_16x16x32_bf16 v[220:223], v[48:51], v[88:91], 0\n\t"
    "ds_read_b64_tr_b16 v[66:67], v157 offset:10240\n\t"
    "v_lshl_add_u32 v10, v3, 3, v10\n\t"
    "v_lshlrev_b32 v10, 6, v10\n\t"
    "v_mfma_f32_16x16x32_bf16 v[224:227], v[52:55], v[76:79], 0\n\t"
    "ds_read_b64_tr_b16 v[68:69], v158 offset:8192\n\t"
    "v_mfma_f32_16x16x32_bf16 v[228:231], v[52:55], v[80:83], 0\n\t"
    "ds_read_b64_tr_b16 v[70:71], v158 offset:10240\n\t"
    "v_mfma_f32_16x16x32_bf16 v[232:235], v[52:55], v[84:87], 0\n\t"
    "ds_read_b64_tr_b16 v[72:73], v159 offset:8192\n\t"
    "buffer_load_dwordx4 v146, s[12:15], s39 offen offset:2048 lds\n\t"
    "v_mfma_f32_16x16x32_bf16 v[236:239], v[52:55], v[88:91], 0\n\t"
    "ds_read_b64_tr_b16 v[74:75], v159 offset:10240\n\t"
    "v_and_b32 v11, 3, v1\n\t"
    "v_lshl_add_u32 v10, v11, 3, v10\n\t"
    "v_mfma_f32_16x16x32_bf16 v[240:243], v[56:59], v[76:79], 0\n\t"
    "v_mfma_f32_16x16x32_bf16 v[244:247], v[56:59], v[80:83], 0\n\t"
    "v_mfma_f32_16x16x32_bf16 v[248:251], v[56:59], v[84:87], 0\n\t"
    "buffer_load_dwordx4 v147, s[12:15], s39 offen offset:3072 lds\n\t"
    "v_mfma_f32_16x16x32_bf16 v[252:255], v[56:59], v[88:91], 0\n\t"
    "s_lshl_b32 s94, s69, 11\n\t"
    "s_add_u32 s94, s94, 131072\n\t"
    "s_waitcnt vmcnt(12)\n\t"
    "s_barrier\n\t"
    "s_waitcnt lgkmcnt(0)\n\t"
    "v_mfma_f32_16x16x32_bf16 v[192:195], v[60:63], v[92:95], v[192:195]\n\t"
    "ds_read_b64_tr_b16 v[44:45], v156 offset:32768\n\t"
    "v_mfma_f32_16x16x32_bf16 v[196:199], v[60:63], v[96:99], v[196:199]\n\t"
    "ds_read_b64_tr_b16 v[46:47], v156 offset:34816\n\t"
    "v_mfma_f32_16x16x32_bf16 v[200:203], v[60:63], v[100:103], v[200:203]\n\t"
    "ds_read_b128 v[76:79], v152 offset:32768\n\t"
    "s_add_u32 m0, s93, 98304\n\t"
    "s_nop 0\n\t"
    "buffer_load_dwordx4 v148, s[16:19], s47 offen offset:0 lds\n\t"
    "v_mfma_f32_16x16x32_bf16 v[204:207], v[60:63], v[104:107], v[204:207]\n\t"
    "ds_read_b128 v[80:83], v152 offset:34816\n\t"
    "v_add_u32 v10, s94, v10\n\t"
    "v_lshlrev_b32 v11, 4, v1\n\t"
    "v_mfma_f32_16x16x32_bf16 v[208:211], v[64:67], v[92:95], v[208:211]\n\t"
    "ds_read_b128 v[84:87], v152 offset:36864\n\t"
    "v_mfma_f32_16x16x32_bf16 v[212:215], v[64:67], v[96:99], v[212:215]\n\t"
    "ds_read_b128 v[88:91], v152 offset:38912\n\t"
    "v_mfma_f32_16x16x32_bf16 v[216:219], v[64:67], v[100:103], v[216:219]\n\t"
    "ds_read_b64_tr_b16 v[48:49], v157 offset:32768\n\t"
    "buffer_load_dwordx4 v149, s[16:19], s47 offen offset:1024 lds\n\t"
    "v_mfma_f32_16x16x32_bf16 v[220:223], v[64:67], v[104:107], v[220:223]\n\t"
    "ds_read_b64_tr_b16 v[50:51], v157 offset:34816\n\t"
    "v_add_u32 v11, s94, v11\n\t"
    "ds_write_b128 v11, v[136:139]\n\t"
    "v_mfma_f32_16x16x32_bf16 v[224:227], v[68:71], v[92:95], v[224:227]\n\t"
    "ds_read_b64_tr_b16 v[52:53], v158 offset:32768\n\t"
    "v_mfma_f32_16x16x32_bf16 v[228:231], v[68:71], v[96:99], v[228:231]\n\t"
    "ds_read_b64_tr_b16 v[54:55], v158 offset:34816\n\t"
    "v_mfma_f32_16x16x32_bf16 v[232:235], v[68:71], v[100:103], v[232:235]\n\t"
    "ds_read_b64_tr_b16 v[56:57], v159 offset:32768\n\t"
    "buffer_load_dwordx4 v150, s[16:19], s47 offen offset:2048 lds\n\t"
    "v_mfma_f32_16x16x32_bf16 v[236:239], v[68:71], v[104:107], v[236:239]\n\t"
    "ds_read_b64_tr_b16 v[58:59], v159 offset:34816\n\t"
    "ds_write_b128 v11, v[140:143] offset:1024\n\t"
    "ds_read_b64_tr_b16 v[136:137], v10 offset:0\n\t"
    "v_mfma_f32_16x16x32_bf16 v[240:243], v[72:75], v[92:95], v[240:243]\n\t"
    "v_mfma_f32_16x16x32_bf16 v[244:247], v[72:75], v[96:99], v[244:247]\n\t"
    "v_mfma_f32_16x16x32_bf16 v[248:251], v[72:75], v[100:103], v[248:251]\n\t"
    "buffer_load_dwordx4 v151, s[16:19], s47 offen offset:3072 lds\n\t"
    "v_mfma_f32_16x16x32_bf16 v[252:255], v[72:75], v[104:107], v[252:255]\n\t"
    "ds_read_b64_tr_b16 v[138:139], v10 offset:256\n\t"
    "ds_read_b64_tr_b16 v[140:141], v10 offset:32\n\t"
    "s_waitcnt lgkmcnt(0)\n\t"
    "v_mfma_f32_16x16x32_bf16 v[192:195], v[44:47], v[76:79], v[192:195]\n\t"
    "ds_read_b64_tr_b16 v[60:61], v156 offset:40960\n\t"
    "v_mfma_f32_16x16x32_bf16 v[196:199], v[44:47], v[80:83], v[196:199]\n\t"
    "ds_read_b64_tr_b16 v[62:63], v156 offset:43008\n\t"
    "v_mfma_f32_16x16x32_bf16 v[200:203], v[44:47], v[84:87], v[200:203]\n\t"
    "ds_read_b128 v[92:95], v153 offset:32768\n\t"
    "v_mfma_f32_16x16x32_bf16 v[204:207], v[44:47], v[88:91], v[204:207]\n\t"
    "ds_read_b128 v[96:99], v153 offset:34816\n\t"
    "v_mfma_f32_16x16x32_bf16 v[208:211], v[48:51], v[76:79], v[208:211]\n\t"
    "ds_read_b128 v[100:103], v153 offset:36864\n\t"
    "v_mfma_f32_16x16x32_bf16 v[212:215], v[48:51], v[80:83], v[212:215]\n\t"
    "ds_read_b128 v[104:107], v153 offset:38912\n\t"
    "ds_read_b64_tr_b16 v[142:143], v10 offset:288\n\t"
    "v_lshlrev_b32 v11, 4, v3\n\t"
    "v_mfma_f32_16x16x32_bf16 v[216:219], v[48:51], v[84:87], v[216:219]\n\t"
    "ds_read_b64_tr_b16 v[64:65], v157 offset:40960\n\t"
    "v_mfma_f32_16x16x32_bf16 v[220:223], v[48:51], v[88:91], v[220:223]\n\t"
    "ds_read_b64_tr_b16 v[66:67], v157 offset:43008\n\t"
    "v_mfma_f32_16x16x32_bf16 v[224:227], v[52:55], v[76:79], v[224:227]\n\t"
    "ds_read_b64_tr_b16 v[68:69], v158 offset:40960\n\t"
    "v_mfma_f32_16x16x32_bf16 v[228:231], v[52:55], v[80:83], v[228:231]\n\t"
    "ds_read_b64_tr_b16 v[70:71], v158 offset:43008\n\t"
    "v_mfma_f32_16x16x32_bf16 v[232:235], v[52:55], v[84:87], v[232:235]\n\t"
    "ds_read_b64_tr_b16 v[72:73], v159 offset:40960\n\t"
    "v_lshl_add_u32 v11, v2, 12, v11\n\t"
    "s_lshl_b32 s96, s71, 17\n\t"
    "v_mfma_f32_16x16x32_bf16 v[236:239], v[52:55], v[88:91], v[236:239]\n\t"
    "ds_read_b64_tr_b16 v[74:75], v159 offset:43008\n\t"
    "v_mfma_f32_16x16x32_bf16 v[240:243], v[56:59], v[76:79], v[240:243]\n\t"
    "v_mfma_f32_16x16x32_bf16 v[244:247], v[56:59], v[80:83], v[244:247]\n\t"
    "v_mfma_f32_16x16x32_bf16 v[248:251], v[56:59], v[84:87], v[248:251]\n\t"
    "v_mfma_f32_16x16x32_bf16 v[252:255], v[56:59], v[88:91], v[252:255]\n\t"
    "s_lshl_b32 s97, s70, 8\n\t"
    "s_add_u32 s96, s96, s97\n\t"
    "s_waitcnt vmcnt(8)\n\t"
    "s_barrier\n\t"
    "s_waitcnt lgkmcnt(0)\n\t"
    "v_mfma_f32_16x16x32_bf16 v[192:195], v[60:63], v[92:95], v[192:195]\n\t"
    "ds_read_b64_tr_b16 v[44:45], v160 offset:0\n\t"
    "v_mfma_f32_16x16x32_bf16 v[196:199], v[60:63], v[96:99], v[196:199]\n\t"
    "ds_read_b64_tr_b16 v[46:47], v160 offset:2048\n\t"
    "v_mfma_f32_16x16x32_bf16 v[200:203], v[60:63], v[100:103], v[200:203]\n\t"
    "ds_read_b128 v[76:79], v154 offset:0\n\t"
    "s_add_u32 m0, s92, 0\n\t"
    "s_nop 0\n\t"
    "buffer_load_dwordx4 v144, s[12:15], s40 offen offset:0 lds\n\t"
    "v_mfma_f32_16x16x32_bf16 v[204:207], v[60:63], v[104:107], v[204:207]\n\t"
    "ds_read_b128 v[80:83], v154 offset:2048\n\t"
    "v_mfma_f32_16x16x32_bf16 v[208:211], v[64:67], v[92:95], v[208:211]\n\t"
    "ds_read_b128 v[84:87], v154 offset:4096\n\t"
    "v_mfma_f32_16x16x32_bf16 v[212:215], v[64:67], v[96:99], v[212:215]\n\t"
    "ds_read_b128 v[88:91], v154 offset:6144\n\t"
    "s_lshl_b32 s97, s69, 6\n\t"
    "s_add_u32 s96, s96, s97\n\t"
    "v_mfma_f32_16x16x32_bf16 v[216:219], v[64:67], v[100:103], v[216:219]\n\t"
    "ds_read_b64_tr_b16 v[48:49], v161 offset:0\n\t"
    "buffer_load_dwordx4 v145, s[12:15], s40 offen offset:1024 lds\n\t"
    "v_mfma_f32_16x16x32_bf16 v[220:223], v[64:67], v[104:107], v[220:223]\n\t"
    "ds_read_b64_tr_b16 v[50:51], v161 offset:2048\n\t"
    "v_mfma_f32_16x16x32_bf16 v[224:227], v[68:71], v[92:95], v[224:227]\n\t"
    "ds_read_b64_tr_b16 v[52:53], v162 offset:0\n\t"
    "v_mfma_f32_16x16x32_bf16 v[228:231], v[68:71], v[96:99], v[228:231]\n\t"
    "ds_read_b64_tr_b16 v[54:55], v162 offset:2048\n\t"
    "v_mfma_f32_16x16x32_bf16 v[232:235], v[68:71], v[100:103], v[232:235]\n\t"
    "ds_read_b64_tr_b16 v[56:57], v163 offset:0\n\t"
    "buffer_load_dwordx4 v146, s[12:15], s40 offen offset:2048 lds\n\t"
    "s_add_u32 s96, s96, 65536\n\t"
    "s_add_u32 s97, s96, 65536\n\t"
    "v_mfma_f32_16x16x32_bf16 v[236:239], v[68:71], v[104:107], v[236:239]\n\t"
    "ds_read_b64_tr_b16 v[58:59], v163 offset:2048\n\t"
    "v_mfma_f32_16x16x32_bf16 v[240:243], v[72:75], v[92:95], v[240:243]\n\t"
    "v_mfma_f32_16x16x32_bf16 v[244:247], v[72:75], v[96:99], v[244:247]\n\t"
    "v_mfma_f32_16x16x32_bf16 v[248:251], v[72:75], v[100:103], v[248:251]\n\t"
    "buffer_load_dwordx4 v147, s[12:15], s40 offen offset:3072 lds\n\t"
    "v_mfma_f32_16x16x32_bf16 v[252:255], v[72:75], v[104:107], v[252:255]\n\t"
    "buffer_store_dwordx4 v[136:139], v11, s[24:27], s96 offen sc1\n\t"
    "buffer_store_dwordx4 v[140:143], v11, s[24:27], s97 offen sc1\n\t"
    "s_waitcnt lgkmcnt(0)\n\t"
    "v_mfma_f32_16x16x32_bf16 v[192:195], v[44:47], v[76:79], v[192:195]\n\t"
    "ds_read_b64_tr_b16 v[60:61], v160 offset:8192\n\t"
    "v_mfma_f32_16x16x32_bf16 v[196:199], v[44:47], v[80:83], v[196:199]\n\t"
    "ds_read_b64_tr_b16 v[62:63], v160 offset:10240\n\t"
    "v_mfma_f32_16x16x32_bf16 v[200:203], v[44:47], v[84:87], v[200:203]\n\t"
    "ds_read_b128 v[92:95], v155 offset:0\n\t"
    "s_add_u32 m0, s93, 0\n\t"
    "s_nop 0\n\t"
    "buffer_load_dwordx4 v148, s[16:19], s48 offen offset:0 lds\n\t"
    "v_mfma_f32_16x16x32_bf16 v[204:207], v[44:47], v[88:91], v[204:207]\n\t"
    "ds_read_b128 v[96:99], v155 offset:2048\n\t"
    "v_lshrrev_b32 v4, 3, v1\n\t"
    "v_and_b32 v5, 6, v4\n\t"
    "v_mfma_f32_16x16x32_bf16 v[208:211], v[48:51], v[76:79], v[208:211]\n\t"
    "ds_read_b128 v[100:103], v155 offset:4096\n\t"
    "v_mfma_f32_16x16x32_bf16 v[212:215], v[48:51], v[80:83], v[212:215]\n\t"
    "ds_read_b128 v[104:107], v155 offset:6144\n\t"
    "v_mfma_f32_16x16x32_bf16 v[216:219], v[48:51], v[84:87], v[216:219]\n\t"
    "ds_read_b64_tr_b16 v[64:65], v161 offset:8192\n\t"
    "buffer_load_dwordx4 v149, s[16:19], s48 offen offset:1024 lds\n\t"
    "v_and_b32 v6, 7, v1\n\t"
    "v_xor_b32 v5, v5, v6\n\t"
    "v_mfma_f32_16x16x32_bf16 v[220:223], v[48:51], v[88:91], v[220:223]\n\t"
    "ds_read_b64_tr_b16 v[66:67], v161 offset:10240\n\t"
    "v_mfma_f32_16x16x32_bf16 v[224:227], v[52:55], v[76:79], v[224:227]\n\t"
    "ds_read_b64_tr_b16 v[68:69], v162 offset:8192\n\t"
    "v_mfma_f32_16x16x32_bf16 v[228:231], v[52:55], v[80:83], v[228:231]\n\t"
    "ds_read_b64_tr_b16 v[70:71], v162 offset:10240\n\t"
    "v_lshlrev_b32 v5, 4, v5\n\t"
    "v_lshl_add_u32 v20, v4, 10, v5\n\t"
    "v_mfma_f32_16x16x32_bf16 v[232:235], v[52:55], v[84:87], v[232:235]\n\t"
    "ds_read_b64_tr_b16 v[72:73], v163 offset:8192\n\t"
    "buffer_load_dwordx4 v150, s[16:19], s48 offen offset:2048 lds\n\t"
    "v_mfma_f32_16x16x32_bf16 v[236:239], v[52:55], v[88:91], v[236:239]\n\t"
    "ds_read_b64_tr_b16 v[74:75], v163 offset:10240\n\t"
    "v_mfma_f32_16x16x32_bf16 v[240:243], v[56:59], v[76:79], v[240:243]\n\t"
    "v_add_u32 v21, 7168, v20\n\t"
    "v_add_u32 v22, 14336, v20\n\t"
    "v_mfma_f32_16x16x32_bf16 v[244:247], v[56:59], v[80:83], v[244:247]\n\t"
    "v_mfma_f32_16x16x32_bf16 v[248:251], v[56:59], v[84:87], v[248:251]\n\t"
    "buffer_load_dwordx4 v151, s[16:19], s48 offen offset:3072 lds\n\t"
    "v_mfma_f32_16x16x32_bf16 v[252:255], v[56:59], v[88:91], v[252:255]\n\t"
    "v_add_u32 v23, 21504, v20\n\t"
    "s_lshl_b32 s87, s69, 12\n\t"
    "s_waitcnt vmcnt(10)\n\t"
    "s_barrier\n\t"
    "s_waitcnt lgkmcnt(0)\n\t"
    "v_mfma_f32_16x16x32_bf16 v[192:195], v[60:63], v[92:95], v[192:195]\n\t"
    "ds_read_b64_tr_b16 v[44:45], v160 offset:32768\n\t"
    "v_mfma_f32_16x16x32_bf16 v[196:199], v[60:63], v[96:99], v[196:199]\n\t"
    "ds_read_b64_tr_b16 v[46:47], v160 offset:34816\n\t"
    "v_mfma_f32_16x16x32_bf16 v[200:203], v[60:63], v[100:103], v[200:203]\n\t"
    "ds_read_b128 v[76:79], v154 offset:32768\n\t"
    "s_add_u32 m0, s92, 32768\n\t"
    "s_nop 0\n\t"
    "buffer_load_dwordx4 v144, s[12:15], s41 offen offset:0 lds\n\t"
    "v_mfma_f32_16x16x32_bf16 v[204:207], v[60:63], v[104:107], v[204:207]\n\t"
    "ds_read_b128 v[80:83], v154 offset:34816\n\t"
    "s_mul_i32 s94, s73, 7\n\t"
    "s_lshr_b32 s94, s94, 2\n\t"
    "v_mfma_f32_16x16x32_bf16 v[208:211], v[64:67], v[92:95], v[208:211]\n\t"
    "ds_read_b128 v[84:87], v154 offset:36864\n\t"
    "v_mfma_f32_16x16x32_bf16 v[212:215], v[64:67], v[96:99], v[212:215]\n\t"
    "ds_read_b128 v[88:91], v154 offset:38912\n\t"
    "v_mfma_f32_16x16x32_bf16 v[216:219], v[64:67], v[100:103], v[216:219]\n\t"
    "ds_read_b64_tr_b16 v[48:49], v161 offset:32768\n\t"
    "buffer_load_dwordx4 v145, s[12:15], s41 offen offset:1024 lds\n\t"
    "s_and_b32 s81, s72, 3\n\t"
    "s_lshl_b32 s81, s81, 2\n\t"
    "v_mfma_f32_16x16x32_bf16 v[220:223], v[64:67], v[104:107], v[220:223]\n\t"
    "ds_read_b64_tr_b16 v[50:51], v161 offset:34816\n\t"
    "v_mfma_f32_16x16x32_bf16 v[224:227], v[68:71], v[92:95], v[224:227]\n\t"
    "ds_read_b64_tr_b16 v[52:53], v162 offset:32768\n\t"
    "v_mfma_f32_16x16x32_bf16 v[228:231], v[68:71], v[96:99], v[228:231]\n\t"
    "ds_read_b64_tr_b16 v[54:55], v162 offset:34816\n\t"
    "s_add_u32 s94, s94, s81\n\t"
    "s_lshr_b32 s81, s72, 2\n\t"
    "v_mfma_f32_16x16x32_bf16 v[232:235], v[68:71], v[100:103], v[232:235]\n\t"
    "ds_read_b64_tr_b16 v[56:57], v163 offset:32768\n\t"
    "buffer_load_dwordx4 v146, s[12:15], s41 offen offset:2048 lds\n\t"
    "v_mfma_f32_16x16x32_bf16 v[236:239], v[68:71], v[104:107], v[236:239]\n\t"
    "ds_read_b64_tr_b16 v[58:59], v163 offset:34816\n\t"
    "v_mfma_f32_16x16x32_bf16 v[240:243], v[72:75], v[92:95], v[240:243]\n\t"
    "s_mul_i32 s81, s81, 5\n\t"
    "s_add_u32 s94, s94, s81\n\t"
    "v_mfma_f32_16x16x32_bf16 v[244:247], v[72:75], v[96:99], v[244:247]\n\t"
    "v_mfma_f32_16x16x32_bf16 v[248:251], v[72:75], v[100:103], v[248:251]\n\t"
    "buffer_load_dwordx4 v147, s[12:15], s41 offen offset:3072 lds\n\t"
    "v_mfma_f32_16x16x32_bf16 v[252:255], v[72:75], v[104:107], v[252:255]\n\t"
    "v_add_u32 v116, s94, v1\n\t"
    "v_cmp_le_u32 vcc, 14, v116\n\t"
    "s_waitcnt lgkmcnt(0)\n\t"
    "v_mfma_f32_16x16x32_bf16 v[192:195], v[44:47], v[76:79], v[192:195]\n\t"
    "ds_read_b64_tr_b16 v[60:61], v160 offset:40960\n\t"
    "v_mfma_f32_16x16x32_bf16 v[196:199], v[44:47], v[80:83], v[196:199]\n\t"
    "ds_read_b64_tr_b16 v[62:63], v160 offset:43008\n\t"
    "v_mfma_f32_16x16x32_bf16 v[200:203], v[44:47], v[84:87], v[200:203]\n\t"
    "ds_read_b128 v[92:95], v155 offset:32768\n\t"
    "s_add_u32 m0, s93, 32768\n\t"
    "s_nop 0\n\t"
    "buffer_load_dwordx4 v148, s[16:19], s49 offen offset:0 lds\n\t"
    "v_mfma_f32_16x16x32_bf16 v[204:207], v[44:47], v[88:91], v[204:207]\n\t"
    "ds_read_b128 v[96:99], v155 offset:34816\n\t"
    "v_subrev_u32 v117, 14, v116\n\t"
    "v_cndmask_b32 v116, v116, v117, vcc\n\t"
    "v_mfma_f32_16x16x32_bf16 v[208:211], v[48:51], v[76:79], v[208:211]\n\t"
    "ds_read_b128 v[100:103], v155 offset:36864\n\t"
    "v_mfma_f32_16x16x32_bf16 v[212:215], v[48:51], v[80:83], v[212:215]\n\t"
    "ds_read_b128 v[104:107], v155 offset:38912\n\t"
    "v_mfma_f32_16x16x32_bf16 v[216:219], v[48:51], v[84:87], v[216:219]\n\t"
    "ds_read_b64_tr_b16 v[64:65], v161 offset:40960\n\t"
    "buffer_load_dwordx4 v149, s[16:19], s49 offen offset:1024 lds\n\t"
    "v_cmp_le_u32 vcc, 14, v116\n\t"
    "v_subrev_u32 v117, 14, v116\n\t"
    "v_mfma_f32_16x16x32_bf16 v[220:223], v[48:51], v[88:91], v[220:223]\n\t"
    "ds_read_b64_tr_b16 v[66:67], v161 offset:43008\n\t"
    "v_mfma_f32_16x16x32_bf16 v[224:227], v[52:55], v[76:79], v[224:227]\n\t"
    "ds_read_b64_tr_b16 v[68:69], v162 offset:40960\n\t"
    "v_mfma_f32_16x16x32_bf16 v[228:231], v[52:55], v[80:83], v[228:231]\n\t"
    "ds_read_b64_tr_b16 v[70:71], v162 offset:43008\n\t"
    "v_cndmask_b32 v116, v116, v117, vcc\n\t"
    "v_cmp_le_u32 vcc, 14, v116\n\t"
    "v_mfma_f32_16x16x32_bf16 v[232:235], v[52:55], v[84:87], v[232:235]\n\t"
    "ds_read_b64_tr_b16 v[72:73], v163 offset:40960\n\t"
    "buffer_load_dwordx4 v150, s[16:19], s49 offen offset:2048 lds\n\t"
    "v_mfma_f32_16x16x32_bf16 v[236:239], v[52:55], v[88:91], v[236:239]\n\t"
    "ds_read_b64_tr_b16 v[74:75], v163 offset:43008\n\t"
    "v_mfma_f32_16x16x32_bf16 v[240:243], v[56:59], v[76:79], v[240:243]\n\t"
    "v_subrev_u32 v117, 14, v116\n\t"
    "v_cndmask_b32 v116, v116, v117, vcc\n\t"
    "v_mfma_f32_16x16x32_bf16 v[244:247], v[56:59], v[80:83], v[244:247]\n\t"
    "v_mfma_f32_16x16x32_bf16 v[248:251], v[56:59], v[84:87], v[248:251]\n\t"
    "buffer_load_dwordx4 v151, s[16:19], s49 offen offset:3072 lds\n\t"
    "v_mfma_f32_16x16x32_bf16 v[252:255], v[56:59], v[88:91], v[252:255]\n\t"
    "v_cmp_le_u32 vcc, 14, v116\n\t"
    "v_subrev_u32 v117, 14, v116\n\t"
    "s_waitcnt vmcnt(8)\n\t"
    "s_barrier\n\t"
    "s_cmp_lg_u32 s69, 0\n\t"
    "s_cbranch_scc1 LGM_fl_3\n\t"
    "s_lshl_b32 s78, s71, 4\n\t"
    "s_add_u32 s78, s78, s70\n\t"
    "s_lshl_b32 s78, s78, 3\n\t"
    "s_add_u32 s78, s78, 4096\n\t"
    "v_mov_b32 v16, s66\n\t"
    "v_mov_b32 v17, s67\n\t"
    "v_mov_b32 v18, 0\n\t"
    "s_mov_b64 exec, 1\n\t"
    "buffer_store_dwordx2 v[16:17], v18, s[24:27], s78 offen sc1\n\t"
    "s_mov_b64 exec, -1\n\t"
    "LGM_fl_3:\n\t"
    "s_waitcnt lgkmcnt(0)\n\t"
    "v_mfma_f32_16x16x32_bf16 v[192:195], v[60:63], v[92:95], v[192:195]\n\t"
    "ds_read_b64_tr_b16 v[44:45], v156 offset:0\n\t"
    "v_mfma_f32_16x16x32_bf16 v[196:199], v[60:63], v[96:99], v[196:199]\n\t"
    "ds_read_b64_tr_b16 v[46:47], v156 offset:2048\n\t"
    "v_mfma_f32_16x16x32_bf16 v[200:203], v[60:63], v[100:103], v[200:203]\n\t"
    "ds_read_b128 v[76:79], v152 offset:0\n\t"
    "s_add_u32 m0, s92, 65536\n\t"
    "s_nop 0\n\t"
    "buffer_load_dwordx4 v144, s[12:15], s42 offen offset:0 lds\n\t"
    "v_mfma_f32_16x16x32_bf16 v[204:207], v[60:63], v[104:107], v[204:207]\n\t"
    "ds_read_b128 v[80:83], v152 offset:2048\n\t"
    "v_cndmask_b32 v116, v116, v117, vcc\n\t"
    "v_cmp_le_u32 vcc, 14, v116\n\t"
    "v_mfma_f32_16x16x32_bf16 v[208:211], v[64:67], v[92:95], v[208:211]\n\t"
    "ds_read_b128 v[84:87], v152 offset:4096\n\t"
    "v_mfma_f32_16x16x32_bf16 v[212:215], v[64:67], v[96:99], v[212:215]\n\t"
    "ds_read_b128 v[88:91], v152 offset:6144\n\t"
    "v_mfma_f32_16x16x32_bf16 v[216:219], v[64:67], v[100:103], v[216:219]\n\t"
    "ds_read_b64_tr_b16 v[48:49], v157 offset:0\n\t"
    "buffer_load_dwordx4 v145, s[12:15], s42 offen offset:1024 lds\n\t"
    "v_subrev_u32 v117, 14, v116\n\t"
    "v_cndmask_b32 v116, v116, v117, vcc\n\t"
    "v_mfma_f32_16x16x32_bf16 v[220:223], v[64:67], v[104:107], v[220:223]\n\t"
    "ds_read_b64_tr_b16 v[50:51], v157 offset:2048\n\t"
    "v_mfma_f32_16x16x32_bf16 v[224:227], v[68:71], v[92:95], v[224:227]\n\t"
    "ds_read_b64_tr_b16 v[52:53], v158 offset:0\n\t"
    "v_mfma_f32_16x16x32_bf16 v[228:231], v[68:71], v[96:99], v[228:231]\n\t"
    "ds_read_b64_tr_b16 v[54:55], v158 offset:2048\n\t"
    "s_mul_i32 s82, s72, 229376\n\t"
    "s_mul_i32 s83, s69, 14336\n\t"
    "v_mfma_f32_16x16x32_bf16 v[232:235], v[68:71], v[100:103], v[232:235]\n\t"
    "ds_read_b64_tr_b16 v[56:57], v159 offset:0\n\t"
    "buffer_load_dwordx4 v146, s[12:15], s42 offen offset:2048 lds\n\t"
    "v_mfma_f32_16x16x32_bf16 v[236:239], v[68:71], v[104:107], v[236:239]\n\t"
    "ds_read_b64_tr_b16 v[58:59], v159 offset:2048\n\t"
    "v_mfma_f32_16x16x32_bf16 v[240:243], v[72:75], v[92:95], v[240:243]\n\t"
    "s_add_u32 s82, s82, s83\n\t"
    "s_add_u32 s82, s82, 4259840\n\t"
    "v_mfma_f32_16x16x32_bf16 v[244:247], v[72:75], v[96:99], v[244:247]\n\t"
    "v_mfma_f32_16x16x32_bf16 v[248:251], v[72:75], v[100:103], v[248:251]\n\t"
    "buffer_load_dwordx4 v147, s[12:15], s42 offen offset:3072 lds\n\t"
    "v_mfma_f32_16x16x32_bf16 v[252:255], v[72:75], v[104:107], v[252:255]\n\t"
    "v_lshlrev_b32 v117, 10, v116\n\t"
    "v_add_u32 v117, s82, v117\n\t"
    "s_waitcnt lgkmcnt(0)\n\t"
    "v_mfma_f32_16x16x32_bf16 v[192:195], v[44:47], v[76:79], v[192:195]\n\t"
    "ds_read_b64_tr_b16 v[60:61], v156 offset:8192\n\t"
    "v_mfma_f32_16x16x32_bf16 v[196:199], v[44:47], v[80:83], v[196:199]\n\t"
    "ds_read_b64_tr_b16 v[62:63], v156 offset:10240\n\t"
    "v_mfma_f32_16x16x32_bf16 v[200:203], v[44:47], v[84:87], v[200:203]\n\t"
    "ds_read_b128 v[92:95], v153 offset:0\n\t"
    "s_add_u32 m0, s93, 65536\n\t"
    "s_nop 0\n\t"
    "buffer_load_dwordx4 v148, s[16:19], s50 offen offset:0 lds\n\t"
    "v_mfma_f32_16x16x32_bf16 v[204:207], v[44:47], v[88:91], v[204:207]\n\t"
    "ds_read_b128 v[96:99], v153 offset:2048\n\t"
    "s_mul_i32 s82, s69, 458752\n\t"
    "s_lshl_b32 s83, s73, 7\n\t"
    "v_mfma_f32_16x16x32_bf16 v[208:211], v[48:51], v[76:79], v[208:211]\n\t"
    "ds_read_b128 v[100:103], v153 offset:4096\n\t"
    "v_mfma_f32_16x16x32_bf16 v[212:215], v[48:51], v[80:83], v[212:215]\n\t"
    "ds_read_b128 v[104:107], v153 offset:6144\n\t"
    "v_mfma_f32_16x16x32_bf16 v[216:219], v[48:51], v[84:87], v[216:219]\n\t"
    "ds_read_b64_tr_b16 v[64:65], v157 offset:8192\n\t"
    "buffer_load_dwordx4 v149, s[16:19], s50 offen offset:1024 lds\n\t"
    "s_add_u32 s82, s82, s83\n\t"
    "v_lshlrev_b32 v119, 15, v116\n\t"
    "v_mfma_f32_16x16x32_bf16 v[220:223], v[48:51], v[88:91], v[220:223]\n\t"
    "ds_read_b64_tr_b16 v[66:67], v157 offset:10240\n\t"
    "v_mfma_f32_16x16x32_bf16 v[224:227], v[52:55], v[76:79], v[224:227]\n\t"
    "ds_read_b64_tr_b16 v[68:69], v158 offset:8192\n\t"
    "v_mfma_f32_16x16x32_bf16 v[228:231], v[52:55], v[80:83], v[228:231]\n\t"
    "ds_read_b64_tr_b16 v[70:71], v158 offset:10240\n\t"
    "v_add_u32 v119, s82, v119\n\t"
    "s_nop 0\n\t"
    "v_mfma_f32_16x16x32_bf16 v[232:235], v[52:55], v[84:87], v[232:235]\n\t"
    "ds_read_b64_tr_b16 v[72:73], v159 offset:8192\n\t"
    "buffer_load_dwordx4 v150, s[16:19], s50 offen offset:2048 lds\n\t"
    "v_mfma_f32_16x16x32_bf16 v[236:239], v[52:55], v[88:91], v[236:239]\n\t"
    "ds_read_b64_tr_b16 v[74:75], v159 offset:10240\n\t"
    "v_mfma_f32_16x16x32_bf16 v[240:243], v[56:59], v[76:79], v[240:243]\n\t"
    "v_readlane_b32 s52, v119, 0\n\t"
    "v_readlane_b32 s53, v119, 1\n\t"
    "v_mfma_f32_16x16x32_bf16 v[244:247], v[56:59], v[80:83], v[244:247]\n\t"
    "v_mfma_f32_16x16x32_bf16 v[248:251], v[56:59], v[84:87], v[248:251]\n\t"
    "buffer_load_dwordx4 v151, s[16:19], s50 offen offset:3072 lds\n\t"
    "v_mfma_f32_16x16x32_bf16 v[252:255], v[56:59], v[88:91], v[252:255]\n\t"
    "v_readlane_b32 s54, v119, 2\n\t"
    "v_readlane_b32 s55, v119, 3\n\t"
    "s_waitcnt vmcnt(8)\n\t"
    "s_barrier\n\t"
    "s_waitcnt lgkmcnt(0)\n\t"
    "v_mfma_f32_16x16x32_bf16 v[192:195], v[60:63], v[92:95], v[192:195]\n\t"
    "ds_read_b64_tr_b16 v[44:45], v156 offset:32768\n\t"
    "v_mfma_f32_16x16x32_bf16 v[196:199], v[60:63], v[96:99], v[196:199]\n\t"
    "ds_read_b64_tr_b16 v[46:47], v156 offset:34816\n\t"
    "v_mfma_f32_16x16x32_bf16 v[200:203], v[60:63], v[100:103], v[200:203]\n\t"
    "ds_read_b128 v[76:79], v152 offset:32768\n\t"
    "s_add_u32 m0, s92, 98304\n\t"
    "s_nop 0\n\t"
    "buffer_load_dwordx4 v144, s[12:15], s43 offen offset:0 lds\n\t"
    "v_mfma_f32_16x16x32_bf16 v[204:207], v[60:63], v[104:107], v[204:207]\n\t"
    "ds_read_b128 v[80:83], v152 offset:34816\n\t"
    "v_readlane_b32 s56, v119, 4\n\t"
    "v_readlane_b32 s57, v119, 5\n\t"
    "v_mfma_f32_16x16x32_bf16 v[208:211], v[64:67], v[92:95], v[208:211]\n\t"
    "ds_read_b128 v[84:87], v152 offset:36864\n\t"
    "v_mfma_f32_16x16x32_bf16 v[212:215], v[64:67], v[96:99], v[212:215]\n\t"
    "ds_read_b128 v[88:91], v152 offset:38912\n\t"
    "v_mfma_f32_16x16x32_bf16 v[216:219], v[64:67], v[100:103], v[216:219]\n\t"
    "ds_read_b64_tr_b16 v[48:49], v157 offset:32768\n\t"
    "buffer_load_dwordx4 v145, s[12:15], s43 offen offset:1024 lds\n\t"
    "v_readlane_b32 s58, v119, 6\n\t"
    "v_readlane_b32 s59, v119, 7\n\t"
    "v_mfma_f32_16x16x32_bf16 v[220:223], v[64:67], v[104:107], v[220:223]\n\t"
    "ds_read_b64_tr_b16 v[50:51], v157 offset:34816\n\t"
    "v_mfma_f32_16x16x32_bf16 v[224:227], v[68:71], v[92:95], v[224:227]\n\t"
    "ds_read_b64_tr_b16 v[52:53], v158 offset:32768\n\t"
    "v_mfma_f32_16x16x32_bf16 v[228:231], v[68:71], v[96:99], v[228:231]\n\t"
    "ds_read_b64_tr_b16 v[54:55], v158 offset:34816\n\t"
    "v_readlane_b32 s60, v119, 8\n\t"
    "v_readlane_b32 s61, v119, 9\n\t"
    "v_mfma_f32_16x16x32_bf16 v[232:235], v[68:71], v[100:103], v[232:235]\n\t"
    "ds_read_b64_tr_b16 v[56:57], v159 offset:32768\n\t"
    "buffer_load_dwordx4 v146, s[12:15], s43 offen offset:2048 lds\n\t"
    "v_mfma_f32_16x16x32_bf16 v[236:239], v[68:71], v[104:107], v[236:239]\n\t"
    "ds_read_b64_tr_b16 v[58:59], v159 offset:34816\n\t"
    "v_mfma_f32_16x16x32_bf16 v[240:243], v[72:75], v[92:95], v[240:243]\n\t"
    "v_readlane_b32 s62, v119, 10\n\t"
    "v_readlane_b32 s63, v119, 11\n\t"
    "v_mfma_f32_16x16x32_bf16 v[244:247], v[72:75], v[96:99], v[244:247]\n\t"
    "v_mfma_f32_16x16x32_bf16 v[248:251], v[72:75], v[100:103], v[248:251]\n\t"
    "buffer_load_dwordx4 v147, s[12:15], s43 offen offset:3072 lds\n\t"
    "v_mfma_f32_16x16x32_bf16 v[252:255], v[72:75], v[104:107], v[252:255]\n\t"
    "v_readlane_b32 s64, v119, 12\n\t"
    "v_readlane_b32 s65, v119, 13\n\t"
    "s_waitcnt lgkmcnt(0)\n\t"
    "v_mfma_f32_16x16x32_bf16 v[192:195], v[44:47], v[76:79], v[192:195]\n\t"
    "ds_read_b64_tr_b16 v[60:61], v156 offset:40960\n\t"
    "v_mfma_f32_16x16x32_bf16 v[196:199], v[44:47], v[80:83], v[196:199]\n\t"
    "ds_read_b64_tr_b16 v[62:63], v156 offset:43008\n\t"
    "v_mfma_f32_16x16x32_bf16 v[200:203], v[44:47], v[84:87], v[200:203]\n\t"
    "ds_read_b128 v[92:95], v153 offset:32768\n\t"
    "s_add_u32 m0, s93, 98304\n\t"
    "s_nop 0\n\t"
    "buffer_load_dwordx4 v148, s[16:19], s51 offen offset:0 lds\n\t"
    "v_mfma_f32_16x16x32_bf16 v[204:207], v[44:47], v[88:91], v[204:207]\n\t"
    "ds_read_b128 v[96:99], v153 offset:34816\n\t"
    "v_mfma_f32_16x16x32_bf16 v[208:211], v[48:51], v[76:79], v[208:211]\n\t"
    "ds_read_b128 v[100:103], v153 offset:36864\n\t"
    "v_mfma_f32_16x16x32_bf16 v[212:215], v[48:51], v[80:83], v[212:215]\n\t"
    "ds_read_b128 v[104:107], v153 offset:38912\n\t"
    "v_mfma_f32_16x16x32_bf16 v[216:219], v[48:51], v[84:87], v[216:219]\n\t"
    "ds_read_b64_tr_b16 v[64:65], v157 offset:40960\n\t"
    "buffer_load_dwordx4 v149, s[16:19], s51 offen offset:1024 lds\n\t"
    "v_mfma_f32_16x16x32_bf16 v[220:223], v[48:51], v[88:91], v[220:223]\n\t"
    "ds_read_b64_tr_b16 v[66:67], v157 offset:43008\n\t"
    "v_mfma_f32_16x16x32_bf16 v[224:227], v[52:55], v[76:79], v[224:227]\n\t"
    "ds_read_b64_tr_b16 v[68:69], v158 offset:40960\n\t"
    "v_mfma_f32_16x16x32_bf16 v[228:231], v[52:55], v[80:83], v[228:231]\n\t"
    "ds_read_b64_tr_b16 v[70:71], v158 offset:43008\n\t"
    "v_mfma_f32_16x16x32_bf16 v[232:235], v[52:55], v[84:87], v[232:235]\n\t"
    "ds_read_b64_tr_b16 v[72:73], v159 offset:40960\n\t"
    "buffer_load_dwordx4 v150, s[16:19], s51 offen offset:2048 lds\n\t"
    "v_mfma_f32_16x16x32_bf16 v[236:239], v[52:55], v[88:91], v[236:239]\n\t"
    "ds_read_b64_tr_b16 v[74:75], v159 offset:43008\n\t"
    "v_mfma_f32_16x16x32_bf16 v[240:243], v[56:59], v[76:79], v[240:243]\n\t"
    "v_mfma_f32_16x16x32_bf16 v[244:247], v[56:59], v[80:83], v[244:247]\n\t"
    "v_mfma_f32_16x16x32_bf16 v[248:251], v[56:59], v[84:87], v[248:251]\n\t"
    "buffer_load_dwordx4 v151, s[16:19], s51 offen offset:3072 lds\n\t"
    "v_mfma_f32_16x16x32_bf16 v[252:255], v[56:59], v[88:91], v[252:255]\n\t"
    "s_waitcnt vmcnt(8)\n\t"
    "s_barrier\n\t"
    "s_waitcnt lgkmcnt(0)\n\t"
    "v_mfma_f32_16x16x32_bf16 v[192:195], v[60:63], v[92:95], v[192:195]\n\t"
    "ds_read_b64_tr_b16 v[44:45], v160 offset:0\n\t"
    "v_mfma_f32_16x16x32_bf16 v[196:199], v[60:63], v[96:99], v[196:199]\n\t"
    "ds_read_b64_tr_b16 v[46:47], v160 offset:2048\n\t"
    "v_mfma_f32_16x16x32_bf16 v[200:203], v[60:63], v[100:103], v[200:203]\n\t"
    "ds_read_b128 v[76:79], v154 offset:0\n\t"
    "v_mfma_f32_16x16x32_bf16 v[204:207], v[60:63], v[104:107], v[204:207]\n\t"
    "ds_read_b128 v[80:83], v154 offset:2048\n\t"
    "v_mfma_f32_16x16x32_bf16 v[208:211], v[64:67], v[92:95], v[208:211]\n\t"
    "ds_read_b128 v[84:87], v154 offset:4096\n\t"
    "v_mfma_f32_16x16x32_bf16 v[212:215], v[64:67], v[96:99], v[212:215]\n\t"
    "ds_read_b128 v[88:91], v154 offset:6144\n\t"
    "v_mfma_f32_16x16x32_bf16 v[216:219], v[64:67], v[100:103], v[216:219]\n\t"
    "ds_read_b64_tr_b16 v[48:49], v161 offset:0\n\t"
    "v_mfma_f32_16x16x32_bf16 v[220:223], v[64:67], v[104:107], v[220:223]\n\t"
    "ds_read_b64_tr_b16 v[50:51], v161 offset:2048\n\t"
    "v_readlane_b32 s36, v117, 0\n\t"
    "v_readlane_b32 s37, v117, 1\n\t"
    "v_mfma_f32_16x16x32_bf16 v[224:227], v[68:71], v[92:95], v[224:227]\n\t"
    "ds_read_b64_tr_b16 v[52:53], v162 offset:0\n\t"
    "v_mfma_f32_16x16x32_bf16 v[228:231], v[68:71], v[96:99], v[228:231]\n\t"
    "ds_read_b64_tr_b16 v[54:55], v162 offset:2048\n\t"
    "v_mfma_f32_16x16x32_bf16 v[232:235], v[68:71], v[100:103], v[232:235]\n\t"
    "ds_read_b64_tr_b16 v[56:57], v163 offset:0\n\t"
    "v_mfma_f32_16x16x32_bf16 v[236:239], v[68:71], v[104:107], v[236:239]\n\t"
    "ds_read_b64_tr_b16 v[58:59], v163 offset:2048\n\t"
    "v_mfma_f32_16x16x32_bf16 v[240:243], v[72:75], v[92:95], v[240:243]\n\t"
    "v_mfma_f32_16x16x32_bf16 v[244:247], v[72:75], v[96:99], v[244:247]\n\t"
    "v_mfma_f32_16x16x32_bf16 v[248:251], v[72:75], v[100:103], v[248:251]\n\t"
    "v_mfma_f32_16x16x32_bf16 v[252:255], v[72:75], v[104:107], v[252:255]\n\t"
    "v_readlane_b32 s38, v117, 2\n\t"
    "v_readlane_b32 s39, v117, 3\n\t"
    "s_waitcnt lgkmcnt(0)\n\t"
    "v_mfma_f32_16x16x32_bf16 v[192:195], v[44:47], v[76:79], v[192:195]\n\t"
    "ds_read_b64_tr_b16 v[60:61], v160 offset:8192\n\t"
    "v_mfma_f32_16x16x32_bf16 v[196:199], v[44:47], v[80:83], v[196:199]\n\t"
    "ds_read_b64_tr_b16 v[62:63], v160 offset:10240\n\t"
    "v_mfma_f32_16x16x32_bf16 v[200:203], v[44:47], v[84:87], v[200:203]\n\t"
    "ds_read_b128 v[92:95], v155 offset:0\n\t"
    "v_mfma_f32_16x16x32_bf16 v[204:207], v[44:47], v[88:91], v[204:207]\n\t"
    "ds_read_b128 v[96:99], v155 offset:2048\n\t"
    "v_mfma_f32_16x16x32_bf16 v[208:211], v[48:51], v[76:79], v[208:211]\n\t"
    "ds_read_b128 v[100:103], v155 offset:4096\n\t"
    "v_mfma_f32_16x16x32_bf16 v[212:215], v[48:51], v[80:83], v[212:215]\n\t"
    "ds_read_b128 v[104:107], v155 offset:6144\n\t"
    "v_mfma_f32_16x16x32_bf16 v[216:219], v[48:51], v[84:87], v[216:219]\n\t"
    "ds_read_b64_tr_b16 v[64:65], v161 offset:8192\n\t"
    "v_mfma_f32_16x16x32_bf16 v[220:223], v[48:51], v[88:91], v[220:223]\n\t"
    "ds_read_b64_tr_b16 v[66:67], v161 offset:10240\n\t"
    "v_readlane_b32 s40, v117, 4\n\t"
    "v_readlane_b32 s41, v117, 5\n\t"
    "v_mfma_f32_16x16x32_bf16 v[224:227], v[52:55], v[76:79], v[224:227]\n\t"
    "ds_read_b64_tr_b16 v[68:69], v162 offset:8192\n\t"
    "s_lshr_b32 s78, s68, 4\n\t"
    "s_lshl_b32 s78, s78, 7\n\t"
    "s_add_u32 s78, s78, 4096\n\t"
    "v_lshlrev_b32 v13, 3, v2\n\t"
    "v_add_u32 v13, s78, v13\n\t"
    "buffer_load_dwordx2 v[14:15], v13, s[24:27], 0 offen sc1\n\t"
    "v_mfma_f32_16x16x32_bf16 v[228:231], v[52:55], v[80:83], v[228:231]\n\t"
    "ds_read_b64_tr_b16 v[70:71], v162 offset:10240\n\t"
    "v_mfma_f32_16x16x32_bf16 v[232:235], v[52:55], v[84:87], v[232:235]\n\t"
    "ds_read_b64_tr_b16 v[72:73], v163 offset:8192\n\t"
    "v_mfma_f32_16x16x32_bf16 v[236:239], v[52:55], v[88:91], v[236:239]\n\t"
    "ds_read_b64_tr_b16 v[74:75], v163 offset:10240\n\t"
    "v_mfma_f32_16x16x32_bf16 v[240:243], v[56:59], v[76:79], v[240:243]\n\t"
    "v_mfma_f32_16x16x32_bf16 v[244:247], v[56:59], v[80:83], v[244:247]\n\t"
    "v_mfma_f32_16x16x32_bf16 v[248:251], v[56:59], v[84:87], v[248:251]\n\t"
    "v_mfma_f32_16x16x32_bf16 v[252:255], v[56:59], v[88:91], v[252:255]\n\t"
    "v_readlane_b32 s42, v117, 6\n\t"
    "v_readlane_b32 s43, v117, 7\n\t"
    "s_waitcnt vmcnt(1)\n\t"
    "s_barrier\n\t"
    "s_waitcnt lgkmcnt(0)\n\t"
    "v_mfma_f32_16x16x32_bf16 v[192:195], v[60:63], v[92:95], v[192:195]\n\t"
    "ds_read_b64_tr_b16 v[44:45], v160 offset:32768\n\t"
    "v_mfma_f32_16x16x32_bf16 v[196:199], v[60:63], v[96:99], v[196:199]\n\t"
    "ds_read_b64_tr_b16 v[46:47], v160 offset:34816\n\t"
    "v_mfma_f32_16x16x32_bf16 v[200:203], v[60:63], v[100:103], v[200:203]\n\t"
    "ds_read_b128 v[76:79], v154 offset:32768\n\t"
    "v_mfma_f32_16x16x32_bf16 v[204:207], v[60:63], v[104:107], v[204:207]\n\t"
    "ds_read_b128 v[80:83], v154 offset:34816\n\t"
    "v_mfma_f32_16x16x32_bf16 v[208:211], v[64:67], v[92:95], v[208:211]\n\t"
    "ds_read_b128 v[84:87], v154 offset:36864\n\t"
    "v_mfma_f32_16x16x32_bf16 v[212:215], v[64:67], v[96:99], v[212:215]\n\t"
    "ds_read_b128 v[88:91], v154 offset:38912\n\t"
    "v_mfma_f32_16x16x32_bf16 v[216:219], v[64:67], v[100:103], v[216:219]\n\t"
    "ds_read_b64_tr_b16 v[48:49], v161 offset:32768\n\t"
    "v_mfma_f32_16x16x32_bf16 v[220:223], v[64:67], v[104:107], v[220:223]\n\t"
    "ds_read_b64_tr_b16 v[50:51], v161 offset:34816\n\t"
    "v_readlane_b32 s44, v117, 8\n\t"
    "v_readlane_b32 s45, v117, 9\n\t"
    "v_mfma_f32_16x16x32_bf16 v[224:227], v[68:71], v[92:95], v[224:227]\n\t"
    "ds_read_b64_tr_b16 v[52:53], v162 offset:32768\n\t"
    "v_mfma_f32_16x16x32_bf16 v[228:231], v[68:71], v[96:99], v[228:231]\n\t"
    "ds_read_b64_tr_b16 v[54:55], v162 offset:34816\n\t"
    "v_mfma_f32_16x16x32_bf16 v[232:235], v[68:71], v[100:103], v[232:235]\n\t"
    "ds_read_b64_tr_b16 v[56:57], v163 offset:32768\n\t"
    "v_mfma_f32_16x16x32_bf16 v[236:239], v[68:71], v[104:107], v[236:239]\n\t"
    "ds_read_b64_tr_b16 v[58:59], v163 offset:34816\n\t"
    "v_mfma_f32_16x16x32_bf16 v[240:243], v[72:75], v[92:95], v[240:243]\n\t"
    "v_mfma_f32_16x16x32_bf16 v[244:247], v[72:75], v[96:99], v[244:247]\n\t"
    "v_mfma_f32_16x16x32_bf16 v[248:251], v[72:75], v[100:103], v[248:251]\n\t"
    "v_mfma_f32_16x16x32_bf16 v[252:255], v[72:75], v[104:107], v[252:255]\n\t"
    "v_readlane_b32 s46, v117, 10\n\t"
    "v_readlane_b32 s47, v117, 11\n\t"
    "s_waitcnt lgkmcnt(0)\n\t"
    "v_mfma_f32_16x16x32_bf16 v[192:195], v[44:47], v[76:79], v[192:195]\n\t"
    "ds_read_b64_tr_b16 v[60:61], v160 offset:40960\n\t"
    "v_mfma_f32_16x16x32_bf16 v[196:199], v[44:47], v[80:83], v[196:199]\n\t"
    "ds_read_b64_tr_b16 v[62:63], v160 offset:43008\n\t"
    "v_readlane_b32 s48, v117, 12\n\t"
    "v_readlane_b32 s49, v117, 13\n\t"
    "v_mfma_f32_16x16x32_bf16 v[200:203], v[44:47], v[84:87], v[200:203]\n\t"
    "ds_read_b128 v[92:95], v155 offset:32768\n\t"
    "v_mfma_f32_16x16x32_bf16 v[204:207], v[44:47], v[88:91], v[204:207]\n\t"
    "ds_read_b128 v[96:99], v155 offset:34816\n\t"
    "v_lshlrev_b32 v12, 4, v1\n\t"
    "s_lshr_b32 s88, s69, 1\n\t"
    "v_mfma_f32_16x16x32_bf16 v[208:211], v[48:51], v[76:79], v[208:211]\n\t"
    "ds_read_b128 v[100:103], v155 offset:36864\n\t"
    "v_mfma_f32_16x16x32_bf16 v[212:215], v[48:51], v[80:83], v[212:215]\n\t"
    "ds_read_b128 v[104:107], v155 offset:38912\n\t"
    "s_mul_i32 s88, s88, 229376\n\t"
    "s_mul_i32 s89, s8, 458752\n\t"
    "v_mfma_f32_16x16x32_bf16 v[216:219], v[48:51], v[84:87], v[216:219]\n\t"
    "ds_read_b64_tr_b16 v[64:65], v161 offset:40960\n\t"
    "v_mfma_f32_16x16x32_bf16 v[220:223], v[48:51], v[88:91], v[220:223]\n\t"
    "ds_read_b64_tr_b16 v[66:67], v161 offset:43008\n\t"
    "s_add_u32 s88, s88, s89\n\t"
    "s_add_u32 s88, s88, 4259840\n\t"
    "v_mfma_f32_16x16x32_bf16 v[224:227], v[52:55], v[76:79], v[224:227]\n\t"
    "ds_read_b64_tr_b16 v[68:69], v162 offset:40960\n\t"
    "v_mfma_f32_16x16x32_bf16 v[228:231], v[52:55], v[80:83], v[228:231]\n\t"
    "ds_read_b64_tr_b16 v[70:71], v162 offset:43008\n\t"
    "s_lshl_b32 s89, s9, 12\n\t"
    "s_add_u32 s88, s88, s89\n\t"
    "v_mfma_f32_16x16x32_bf16 v[232:235], v[52:55], v[84:87], v[232:235]\n\t"
    "ds_read_b64_tr_b16 v[72:73], v163 offset:40960\n\t"
    "v_mfma_f32_16x16x32_bf16 v[236:239], v[52:55], v[88:91], v[236:239]\n\t"
    "ds_read_b64_tr_b16 v[74:75], v163 offset:43008\n\t"
    "s_and_b32 s89, s69, 1\n\t"
    "s_lshl_b32 s89, s89, 11\n\t"
    "v_mfma_f32_16x16x32_bf16 v[240:243], v[56:59], v[76:79], v[240:243]\n\t"
    "v_mfma_f32_16x16x32_bf16 v[244:247], v[56:59], v[80:83], v[244:247]\n\t"
    "s_add_u32 s88, s88, s89\n\t"
    "s_add_u32 s89, s88, 57344\n\t"
    "v_mfma_f32_16x16x32_bf16 v[248:251], v[56:59], v[84:87], v[248:251]\n\t"
    "v_mfma_f32_16x16x32_bf16 v[252:255], v[56:59], v[88:91], v[252:255]\n\t"
    "s_add_u32 s90, s89, 57344\n\t"
    "s_add_u32 s91, s90, 57344\n\t"
    "s_waitcnt lgkmcnt(0)\n\t"
    "s_waitcnt vmcnt(0)\n\t"
    "v_cmp_eq_u64 vcc, s[66:67], v[14:15]\n\t"
    "s_nop 1\n\t"
    "s_cmp_eq_u64 vcc, -1\n\t"
    "s_cbranch_scc1 LGM_p1ok_4\n\t"
    "s_mov_b32 s77, 0\n\t"
    "LGM_p1_spin_5:\n\t"
    "buffer_load_dwordx2 v[14:15], v13, s[24:27], 0 offen sc1\n\t"
    "s_waitcnt vmcnt(0)\n\t"
    "v_cmp_eq_u64 vcc, s[66:67], v[14:15]\n\t"
    "s_nop 1\n\t"
    "s_cmp_eq_u64 vcc, -1\n\t"
    "s_cbranch_scc1 LGM_p1_done_6\n\t"
    "s_add_u32 s77, s77, 1\n\t"
    "s_cmp_gt_u32 s77, 20000\n\t"
    "s_cbranch_scc1 LGM_p1_done_6\n\t"
    "s_sleep 1\n\t"
    "s_branch LGM_p1_spin_5\n\t"
    "LGM_p1_done_6:\n\t"
    "LGM_p1ok_4:\n\t"
    "v_lshlrev_b32 v6, 4, v1\n\t"
    "s_lshl_b32 s78, s74, 12\n\t"
    "s_and_b32 s79, s69, 1\n\t"
    "s_lshl_b32 s79, s79, 11\n\t"
    "s_add_u32 s78, s78, s79\n\t"
    "s_add_u32 s78, s78, 65536\n\t"
    "buffer_load_dwordx4 v[24:27], v6, s[24:27], s78 offen sc1\n\t"
    "buffer_load_dwordx4 v[28:31], v6, s[24:27], s78 offen offset:1024 sc1\n\t"
    "v_mfma_f32_16x16x32_bf16 v[192:195], v[60:63], v[92:95], v[192:195]\n\t"
    "v_mfma_f32_16x16x32_bf16 v[196:199], v[60:63], v[96:99], v[196:199]\n\t"
    "v_mfma_f32_16x16x32_bf16 v[200:203], v[60:63], v[100:103], v[200:203]\n\t"
    "v_mfma_f32_16x16x32_bf16 v[204:207], v[60:63], v[104:107], v[204:207]\n\t"
    "v_mfma_f32_16x16x32_bf16 v[208:211], v[64:67], v[92:95], v[208:211]\n\t"
    "v_mfma_f32_16x16x32_bf16 v[212:215], v[64:67], v[96:99], v[212:215]\n\t"
    "v_mfma_f32_16x16x32_bf16 v[216:219], v[64:67], v[100:103], v[216:219]\n\t"
    "v_mfma_f32_16x16x32_bf16 v[220:223], v[64:67], v[104:107], v[220:223]\n\t"
    "v_mfma_f32_16x16x32_bf16 v[224:227], v[68:71], v[92:95], v[224:227]\n\t"
    "v_mfma_f32_16x16x32_bf16 v[228:231], v[68:71], v[96:99], v[228:231]\n\t"
    "v_mfma_f32_16x16x32_bf16 v[232:235], v[68:71], v[100:103], v[232:235]\n\t"
    "v_mfma_f32_16x16x32_bf16 v[236:239], v[68:71], v[104:107], v[236:239]\n\t"
    "v_mfma_f32_16x16x32_bf16 v[240:243], v[72:75], v[92:95], v[240:243]\n\t"
    "v_cvt_pk_bf16_f32 v108, v192, v193\n\t"
    "v_cvt_pk_bf16_f32 v109, v194, v195\n\t"
    "v_cvt_pk_bf16_f32 v110, v208, v209\n\t"
    "v_cvt_pk_bf16_f32 v111, v210, v211\n\t"
    "v_pk_max_i16 v108, v108, 0\n\t"
    "v_pk_max_i16 v109, v109, 0\n\t"
    "v_pk_max_i16 v110, v110, 0\n\t"
    "v_pk_max_i16 v111, v111, 0\n\t"
    "buffer_store_dwordx4 v[108:111], v12, s[24:27], s88 offen offset:0 sc1\n\t"
    "v_mfma_f32_16x16x32_bf16 v[244:247], v[72:75], v[96:99], v[244:247]\n\t"
    "v_cvt_pk_bf16_f32 v116, v196, v197\n\t"
    "v_cvt_pk_bf16_f32 v117, v198, v199\n\t"
    "v_cvt_pk_bf16_f32 v118, v212, v213\n\t"
    "v_cvt_pk_bf16_f32 v119, v214, v215\n\t"
    "v_pk_max_i16 v116, v116, 0\n\t"
    "v_pk_max_i16 v117, v117, 0\n\t"
    "v_pk_max_i16 v118, v118, 0\n\t"
    "v_pk_max_i16 v119, v119, 0\n\t"
    "buffer_store_dwordx4 v[116:119], v12, s[24:27], s89 offen offset:0 sc1\n\t"
    "v_mfma_f32_16x16x32_bf16 v[248:251], v[72:75], v[100:103], v[248:251]\n\t"
    "v_cvt_pk_bf16_f32 v124, v200, v201\n\t"
    "v_cvt_pk_bf16_f32 v125, v202, v203\n\t"
    "v_cvt_pk_bf16_f32 v126, v216, v217\n\t"
    "v_cvt_pk_bf16_f32 v127, v218, v219\n\t"
    "v_pk_max_i16 v124, v124, 0\n\t"
    "v_pk_max_i16 v125, v125, 0\n\t"
    "v_pk_max_i16 v126, v126, 0\n\t"
    "v_pk_max_i16 v127, v127, 0\n\t"
    "buffer_store_dwordx4 v[124:127], v12, s[24:27], s90 offen offset:0 sc1\n\t"
    "v_mfma_f32_16x16x32_bf16 v[252:255], v[72:75], v[104:107], v[252:255]\n\t"
    "v_cvt_pk_bf16_f32 v132, v204, v205\n\t"
    "v_cvt_pk_bf16_f32 v133, v206, v207\n\t"
    "v_cvt_pk_bf16_f32 v134, v220, v221\n\t"
    "v_cvt_pk_bf16_f32 v135, v222, v223\n\t"
    "v_pk_max_i16 v132, v132, 0\n\t"
    "v_pk_max_i16 v133, v133, 0\n\t"
    "v_pk_max_i16 v134, v134, 0\n\t"
    "v_pk_max_i16 v135, v135, 0\n\t"
    "buffer_store_dwordx4 v[132:135], v12, s[24:27], s91 offen offset:0 sc1\n\t"
    "s_barrier\n\t"
    "; h epilogue\n\t"
    "v_cvt_pk_bf16_f32 v112, v224, v225\n\t"
    "v_cvt_pk_bf16_f32 v113, v226, v227\n\t"
    "v_cvt_pk_bf16_f32 v114, v240, v241\n\t"
    "v_cvt_pk_bf16_f32 v115, v242, v243\n\t"
    "v_pk_max_i16 v112, v112, 0\n\t"
    "v_pk_max_i16 v113, v113, 0\n\t"
    "v_pk_max_i16 v114, v114, 0\n\t"
    "v_pk_max_i16 v115, v115, 0\n\t"
    "buffer_store_dwordx4 v[112:115], v12, s[24:27], s88 offen offset:1024 sc1\n\t"
    "v_cvt_pk_bf16_f32 v120, v228, v229\n\t"
    "v_cvt_pk_bf16_f32 v121, v230, v231\n\t"
    "v_cvt_pk_bf16_f32 v122, v244, v245\n\t"
    "v_cvt_pk_bf16_f32 v123, v246, v247\n\t"
    "v_pk_max_i16 v120, v120, 0\n\t"
    "v_pk_max_i16 v121, v121, 0\n\t"
    "v_pk_max_i16 v122, v122, 0\n\t"
    "v_pk_max_i16 v123, v123, 0\n\t"
    "buffer_store_dwordx4 v[120:123], v12, s[24:27], s89 offen offset:1024 sc1\n\t"
    "v_cvt_pk_bf16_f32 v128, v232, v233\n\t"
    "v_cvt_pk_bf16_f32 v129, v234, v235\n\t"
    "v_cvt_pk_bf16_f32 v130, v248, v249\n\t"
    "v_cvt_pk_bf16_f32 v131, v250, v251\n\t"
    "v_pk_max_i16 v128, v128, 0\n\t"
    "v_pk_max_i16 v129, v129, 0\n\t"
    "v_pk_max_i16 v130, v130, 0\n\t"
    "v_pk_max_i16 v131, v131, 0\n\t"
    "buffer_store_dwordx4 v[128:131], v12, s[24:27], s90 offen offset:1024 sc1\n\t"
    "v_cvt_pk_bf16_f32 v136, v236, v237\n\t"
    "v_cvt_pk_bf16_f32 v137, v238, v239\n\t"
    "v_cvt_pk_bf16_f32 v138, v252, v253\n\t"
    "v_cvt_pk_bf16_f32 v139, v254, v255\n\t"
    "v_pk_max_i16 v136, v136, 0\n\t"
    "v_pk_max_i16 v137, v137, 0\n\t"
    "v_pk_max_i16 v138, v138, 0\n\t"
    "v_pk_max_i16 v139, v139, 0\n\t"
    "buffer_store_dwordx4 v[136:139], v12, s[24:27], s91 offen offset:1024 sc1\n\t"
    "s_branch LGM_g1j_2\n\t"
    "LGM_inact_1:\n\t"
    "; transpose of the u slice\n\t"
    "v_lshrrev_b32 v10, 2, v2\n\t"
    "v_lshl_add_u32 v10, v3, 3, v10\n\t"
    "v_lshlrev_b32 v10, 6, v10\n\t"
    "v_and_b32 v11, 3, v1\n\t"
    "v_lshl_add_u32 v10, v11, 3, v10\n\t"
    "s_lshl_b32 s94, s69, 11\n\t"
    "s_add_u32 s94, s94, 131072\n\t"
    "v_add_u32 v10, s94, v10\n\t"
    "v_lshlrev_b32 v11, 4, v1\n\t"
    "v_add_u32 v11, s94, v11\n\t"
    "s_waitcnt vmcnt(25)\n\t"
    "ds_write_b128 v11, v[136:139]\n\t"
    "s_waitcnt vmcnt(24)\n\t"
    "ds_write_b128 v11, v[140:143] offset:1024\n\t"
    "ds_read_b64_tr_b16 v[136:137], v10 offset:0\n\t"
    "ds_read_b64_tr_b16 v[138:139], v10 offset:256\n\t"
    "ds_read_b64_tr_b16 v[140:141], v10 offset:32\n\t"
    "ds_read_b64_tr_b16 v[142:143], v10 offset:288\n\t"
    "v_lshlrev_b32 v11, 4, v3\n\t"
    "v_lshl_add_u32 v11, v2, 12, v11\n\t"
    "s_lshl_b32 s96, s71, 17\n\t"
    "s_lshl_b32 s97, s70, 8\n\t"
    "s_add_u32 s96, s96, s97\n\t"
    "s_lshl_b32 s97, s69, 6\n\t"
    "s_add_u32 s96, s96, s97\n\t"
    "s_add_u32 s96, s96, 65536\n\t"
    "s_add_u32 s97, s96, 65536\n\t"
    "s_waitcnt lgkmcnt(2)\n\t"
    "buffer_store_dwordx4 v[136:139], v11, s[24:27], s96 offen sc1\n\t"
    "s_waitcnt lgkmcnt(0)\n\t"
    "buffer_store_dwordx4 v[140:143], v11, s[24:27], s97 offen sc1\n\t"
    "s_waitcnt vmcnt(0)\n\t"
    "s_barrier\n\t"
    "s_cmp_lg_u32 s69, 0\n\t"
    "s_cbranch_scc1 LGM_fl_7\n\t"
    "s_lshl_b32 s78, s71, 4\n\t"
    "s_add_u32 s78, s78, s70\n\t"
    "s_lshl_b32 s78, s78, 3\n\t"
    "s_add_u32 s78, s78, 4096\n\t"
    "v_mov_b32 v16, s66\n\t"
    "v_mov_b32 v17, s67\n\t"
    "v_mov_b32 v18, 0\n\t"
    "s_mov_b64 exec, 1\n\t"
    "buffer_store_dwordx2 v[16:17], v18, s[24:27], s78 offen sc1\n\t"
    "s_mov_b64 exec, -1\n\t"
    "LGM_fl_7:\n\t"
    "v_lshrrev_b32 v4, 3, v1\n\t"
    "v_and_b32 v5, 6, v4\n\t"
    "v_and_b32 v6, 7, v1\n\t"
    "v_xor_b32 v5, v5, v6\n\t"
    "v_lshlrev_b32 v5, 4, v5\n\t"
    "v_lshl_add_u32 v20, v4, 10, v5\n\t"
    "v_add_u32 v21, 7168, v20\n\t"
    "v_add_u32 v22, 14336, v20\n\t"
    "v_add_u32 v23, 21504, v20\n\t"
    "s_lshl_b32 s87, s69, 12\n\t"
    "s_mul_i32 s94, s73, 7\n\t"
    "s_lshr_b32 s94, s94, 2\n\t"
    "s_and_b32 s81, s72, 3\n\t"
    "s_lshl_b32 s81, s81, 2\n\t"
    "s_add_u32 s94, s94, s81\n\t"
    "s_lshr_b32 s81, s72, 2\n\t"
    "s_mul_i32 s81, s81, 5\n\t"
    "s_add_u32 s94, s94, s81\n\t"
    "v_add_u32 v116, s94, v1\n\t"
    "v_cmp_le_u32 vcc, 14, v116\n\t"
    "v_subrev_u32 v117, 14, v116\n\t"
    "s_nop 0\n\t"
    "v_cndmask_b32 v116, v116, v117, vcc\n\t"
    "v_cmp_le_u32 vcc, 14, v116\n\t"
    "v_subrev_u32 v117, 14, v116\n\t"
    "s_nop 0\n\t"
    "v_cndmask_b32 v116, v116, v117, vcc\n\t"
    "v_cmp_le_u32 vcc, 14, v116\n\t"
    "v_subrev_u32 v117, 14, v116\n\t"
    "s_nop 0\n\t"
    "v_cndmask_b32 v116, v116, v117, vcc\n\t"
    "v_cmp_le_u32 vcc, 14, v116\n\t"
    "v_subrev_u32 v117, 14, v116\n\t"
    "s_nop 0\n\t"
    "v_cndmask_b32 v116, v116, v117, vcc\n\t"
    "v_cmp_le_u32 vcc, 14, v116\n\t"
    "v_subrev_u32 v117, 14, v116\n\t"
    "s_nop 0\n\t"
    "v_cndmask_b32 v116, v116, v117, vcc\n\t"
    "s_mul_i32 s82, s72, 229376\n\t"
    "s_mul_i32 s83, s69, 14336\n\t"
    "s_add_u32 s82, s82, s83\n\t"
    "s_add_u32 s82, s82, 4259840\n\t"
    "v_lshlrev_b32 v117, 10, v116\n\t"
    "v_add_u32 v117, s82, v117\n\t"
    "s_mul_i32 s82, s69, 458752\n\t"
    "s_lshl_b32 s83, s73, 7\n\t"
    "s_add_u32 s82, s82, s83\n\t"
    "v_lshlrev_b32 v119, 15, v116\n\t"
    "v_add_u32 v119, s82, v119\n\t"
    "s_nop 0\n\t"
    "v_readlane_b32 s52, v119, 0\n\t"
    "v_readlane_b32 s53, v119, 1\n\t"
    "v_readlane_b32 s54, v119, 2\n\t"
    "v_readlane_b32 s55, v119, 3\n\t"
    "v_readlane_b32 s56, v119, 4\n\t"
    "v_readlane_b32 s57, v119, 5\n\t"
    "v_readlane_b32 s58, v119, 6\n\t"
    "v_readlane_b32 s59, v119, 7\n\t"
    "v_readlane_b32 s60, v119, 8\n\t"
    "v_readlane_b32 s61, v119, 9\n\t"
    "v_readlane_b32 s62, v119, 10\n\t"
    "v_readlane_b32 s63, v119, 11\n\t"
    "v_readlane_b32 s64, v119, 12\n\t"
    "v_readlane_b32 s65, v119, 13\n\t"
    "v_readlane_b32 s36, v117, 0\n\t"
    "v_readlane_b32 s37, v117, 1\n\t"
    "v_readlane_b32 s38, v117, 2\n\t"
    "v_readlane_b32 s39, v117, 3\n\t"
    "v_readlane_b32 s40, v117, 4\n\t"
    "v_readlane_b32 s41, v117, 5\n\t"
    "v_readlane_b32 s42, v117, 6\n\t"
    "v_readlane_b32 s43, v117, 7\n\t"
    "v_readlane_b32 s44, v117, 8\n\t"
    "v_readlane_b32 s45, v117, 9\n\t"
    "v_readlane_b32 s46, v117, 10\n\t"
    "v_readlane_b32 s47, v117, 11\n\t"
    "v_readlane_b32 s48, v117, 12\n\t"
    "v_readlane_b32 s49, v117, 13\n\t"
    "s_sleep 70\n\t"
    "s_lshr_b32 s78, s68, 4\n\t"
    "s_lshl_b32 s78, s78, 7\n\t"
    "s_add_u32 s78, s78, 4096\n\t"
    "v_lshlrev_b32 v13, 3, v2\n\t"
    "v_add_u32 v13, s78, v13\n\t"
    "buffer_load_dwordx2 v[14:15], v13, s[24:27], 0 offen sc1\n\t"
    "s_waitcnt vmcnt(0)\n\t"
    "v_cmp_eq_u64 vcc, s[66:67], v[14:15]\n\t"
    "s_nop 1\n\t"
    "s_cmp_eq_u64 vcc, -1\n\t"
    "s_cbranch_scc1 LGM_p1ok_8\n\t"
    "s_mov_b32 s77, 0\n\t"
    "LGM_p1_spin_9:\n\t"
    "buffer_load_dwordx2 v[14:15], v13, s[24:27], 0 offen sc1\n\t"
    "s_waitcnt vmcnt(0)\n\t"
    "v_cmp_eq_u64 vcc, s[66:67], v[14:15]\n\t"
    "s_nop 1\n\t"
    "s_cmp_eq_u64 vcc, -1\n\t"
    "s_cbranch_scc1 LGM_p1_done_10\n\t"
    "s_add_u32 s77, s77, 1\n\t"
    "s_cmp_gt_u32 s77, 20000\n\t"
    "s_cbranch_scc1 LGM_p1_done_10\n\t"
    "s_sleep 1\n\t"
    "s_branch LGM_p1_spin_9\n\t"
    "LGM_p1_done_10:\n\t"
    "LGM_p1ok_8:\n\t"
    "v_lshlrev_b32 v6, 4, v1\n\t"
    "s_lshl_b32 s78, s74, 12\n\t"
    "s_and_b32 s79, s69, 1\n\t"
    "s_lshl_b32 s79, s79, 11\n\t"
    "s_add_u32 s78, s78, s79\n\t"
    "s_add_u32 s78, s78, 65536\n\t"
    "buffer_load_dwordx4 v[24:27], v6, s[24:27], s78 offen sc1\n\t"
    "buffer_load_dwordx4 v[28:31], v6, s[24:27], s78 offen offset:1024 sc1\n\t"
    "s_waitcnt vmcnt(0) lgkmcnt(0)\n\t"
    "LGM_g1j_2:\n\t"
    "v_and_b32 v208, 31, v1\n\t"
    "v_lshlrev_b32 v208, 5, v208\n\t"
    "v_lshrrev_b32 v209, 5, v1\n\t"
    "v_lshl_add_u32 v209, v209, 10, v208\n\t"
    "s_lshl_b32 s79, s71, 3\n\t"
    "s_lshl_b32 s80, s69, 1\n\t"
    "s_add_u32 s79, s79, s80\n\t"
    "s_lshl_b32 s79, s79, 10\n\t"
    "s_add_u32 s79, s79, s84\n\t"
    "buffer_load_dwordx4 v[192:195], v209, s[12:15], s79 offen\n\t"
    "buffer_load_dwordx4 v[196:199], v209, s[12:15], s79 offen offset:16\n\t"
    "buffer_load_dwordx4 v[200:203], v208, s[32:35], 0 offen\n\t"
    "buffer_load_dwordx4 v[204:207], v208, s[32:35], 0 offen offset:16\n\t"
    "; uT -> LDS, K expansion\n\t"
    "; K: partial tables -> expanded bf16 copy 0\n\t"
    "v_mov_b32 v16, 0\n\t"
    "v_mov_b32 v17, 0\n\t"
    "v_mov_b32 v18, 0\n\t"
    "v_mov_b32 v19, 0\n\t"
    "v_lshrrev_b32 v13, 3, v2\n\t"
    "v_mul_u32_u24 v13, 16640, v13\n\t"
    "v_bfe_u32 v14, v1, 1, 2\n\t"
    "v_mul_u32_u24 v14, 4160, v14\n\t"
    "v_and_b32 v15, 1, v1\n\t"
    "v_lshlrev_b32 v15, 4, v15\n\t"
    "v_add3_u32 v13, v13, v14, v15\n\t"
    "v_add_u32 v13, 120832, v13\n\t"
    "ds_write_b128 v13, v[16:19]\n\t"
    "s_lshr_b32 s79, s69, 1\n\t"
    "s_and_b32 s80, s69, 1\n\t"
    "s_lshl_b32 s80, s80, 6\n\t"
    "s_add_u32 s80, s80, 128\n\t"
    "v_mov_b32 v13, s79\n\t"
    "v_add_u32 v12, s80, v1\n\t"
    "v_mul_u32_u24 v14, 6144, v13\n\t"
    "v_mul_u32_u24 v6, 4, v12\n\t"
    "v_add_u32 v6, v6, v14\n\t"
    "v_add_u32 v6, 153088, v6\n\t"
    "v_mul_u32_u24 v14, 16640, v13\n\t"
    "v_lshlrev_b32 v7, 4, v12\n\t"
    "v_sub_u32 v7, v14, v7\n\t"
    "v_add_u32 v7, 120816, v7\n\t"
    "ds_read2_b32 v[44:45], v6 offset0:0 offset1:1\n\t"
    "v_add_u32 v15, 3072, v6\n\t"
    "ds_read2_b32 v[52:53], v15 offset0:0 offset1:1\n\t"
    "s_waitcnt lgkmcnt(0)\n\t"
    "v_add_f32 v44, v44, v52\n\t"
    "v_add_f32 v45, v45, v53\n\t"
    "v_and_b32 v14, 1, v12\n\t"
    "v_cmp_eq_u32 vcc, 0, v14\n\t"
    "s_nop 1\n\t"
    "v_cndmask_b32 v46, v45, v44, vcc\n\t"
    "v_cvt_pk_bf16_f32 v64, v45, v45\n\t"
    "v_cvt_pk_bf16_f32 v65, v45, v46\n\t"
    "v_cvt_pk_bf16_f32 v66, v44, v44\n\t"
    "v_cvt_pk_bf16_f32 v67, v44, v44\n\t"
    "ds_write_b128 v7, v[64:67]\n\t"
    "s_waitcnt lgkmcnt(0)\n\t"
    "s_cmp_eq_u32 s69, 1\n\t"
    "s_cbranch_scc1 LGM_r1_11\n\t"
    "s_cmp_gt_u32 s69, 1\n\t"
    "s_cbranch_scc1 LGM_r2_12\n\t"
    "v_lshrrev_b32 v13, 5, v1\n\t"
    "v_and_b32 v12, 31, v1\n\t"
    "v_mul_u32_u24 v14, 6144, v13\n\t"
    "v_mul_u32_u24 v6, 32, v12\n\t"
    "v_add_u32 v6, v6, v14\n\t"
    "v_add_u32 v6, 151552, v6\n\t"
    "v_mul_u32_u24 v14, 16640, v13\n\t"
    "v_lshlrev_b32 v7, 4, v12\n\t"
    "v_sub_u32 v7, v14, v7\n\t"
    "v_add_u32 v7, 120816, v7\n\t"
    "ds_read_b128 v[44:47], v6 offset:0\n\t"
    "ds_read_b128 v[48:51], v6 offset:16\n\t"
    "ds_read_b128 v[52:55], v6 offset:3072\n\t"
    "ds_read_b128 v[56:59], v6 offset:3088\n\t"
    "s_waitcnt lgkmcnt(1)\n\t"
    "v_add_f32 v44, v44, v52\n\t"
    "v_add_f32 v45, v45, v53\n\t"
    "v_add_f32 v46, v46, v54\n\t"
    "v_add_f32 v47, v47, v55\n\t"
    "s_waitcnt lgkmcnt(0)\n\t"
    "v_add_f32 v48, v48, v56\n\t"
    "v_add_f32 v49, v49, v57\n\t"
    "v_add_f32 v50, v50, v58\n\t"
    "v_add_f32 v51, v51, v59\n\t"
    "v_cvt_pk_bf16_f32 v64, v51, v50\n\t"
    "v_cvt_pk_bf16_f32 v65, v49, v48\n\t"
    "v_cvt_pk_bf16_f32 v66, v47, v46\n\t"
    "v_cvt_pk_bf16_f32 v67, v45, v44\n\t"
    "ds_write_b128 v7, v[64:67]\n\t"
    "s_waitcnt lgkmcnt(0)\n\t"
    "s_branch LGM_rj_13\n\t"
    "LGM_r1_11:\n\t"
    "v_lshrrev_b32 v13, 5, v1\n\t"
    "v_and_b32 v12, 31, v1\n\t"
    "v_add_u32 v12, 32, v12\n\t"
    "v_mul_u32_u24 v14, 6144, v13\n\t"
    "v_mul_u32_u24 v6, 16, v12\n\t"
    "v_add_u32 v6, v6, v14\n\t"
    "v_add_u32 v6, 152064, v6\n\t"
    "v_mul_u32_u24 v14, 16640, v13\n\t"
    "v_lshlrev_b32 v7, 4, v12\n\t"
    "v_sub_u32 v7, v14, v7\n\t"
    "v_add_u32 v7, 120816, v7\n\t"
    "ds_read_b128 v[44:47], v6 offset:0\n\t"
    "ds_read_b32 v48, v6 offset:16\n\t"
    "ds_read_b128 v[52:55], v6 offset:3072\n\t"
    "ds_read_b32 v56, v6 offset:3088\n\t"
    "s_waitcnt lgkmcnt(1)\n\t"
    "v_add_f32 v44, v44, v52\n\t"
    "v_add_f32 v45, v45, v53\n\t"
    "v_add_f32 v46, v46, v54\n\t"
    "v_add_f32 v47, v47, v55\n\t"
    "s_waitcnt lgkmcnt(0)\n\t"
    "v_add_f32 v48, v48, v56\n\t"
    "v_cvt_pk_bf16_f32 v64, v48, v47\n\t"
    "v_cvt_pk_bf16_f32 v65, v46, v46\n\t"
    "v_cvt_pk_bf16_f32 v66, v46, v45\n\t"
    "v_cvt_pk_bf16_f32 v67, v44, v44\n\t"
    "ds_write_b128 v7, v[64:67]\n\t"
    "s_waitcnt lgkmcnt(0)\n\t"
    "s_branch LGM_rj_13\n\t"
    "LGM_r2_12:\n\t"
    "s_sub_u32 s79, s69, 2\n\t"
    "v_mov_b32 v13, s79\n\t"
    "v_add_u32 v12, 64, v1\n\t"
    "v_mul_u32_u24 v14, 6144, v13\n\t"
    "v_mul_u32_u24 v6, 8, v12\n\t"
    "v_add_u32 v6, v6, v14\n\t"
    "v_add_u32 v6, 152576, v6\n\t"
    "v_mul_u32_u24 v14, 16640, v13\n\t"
    "v_lshlrev_b32 v7, 4, v12\n\t"
    "v_sub_u32 v7, v14, v7\n\t"
    "v_add_u32 v7, 120816, v7\n\t"
    "ds_read_b64 v[44:45], v6 offset:0\n\t"
    "ds_read_b32 v46, v6 offset:8\n\t"
    "ds_read_b64 v[52:53], v6 offset:3072\n\t"
    "ds_read_b32 v54, v6 offset:3080\n\t"
    "s_waitcnt lgkmcnt(1)\n\t"
    "v_add_f32 v44, v44, v52\n\t"
    "v_add_f32 v45, v45, v53\n\t"
    "s_waitcnt lgkmcnt(0)\n\t"
    "v_add_f32 v46, v46, v54\n\t"
    "v_cvt_pk_bf16_f32 v64, v46, v46\n\t"
    "v_cvt_pk_bf16_f32 v65, v45, v45\n\t"
    "v_cvt_pk_bf16_f32 v66, v45, v44\n\t"
    "v_cvt_pk_bf16_f32 v67, v44, v44\n\t"
    "ds_write_b128 v7, v[64:67]\n\t"
    "s_waitcnt lgkmcnt(0)\n\t"
    "LGM_rj_13:\n\t"
    "s_barrier\n\t"
    "s_lshr_b32 s79, s69, 1\n\t"
    "s_mul_i32 s79, s79, 16640\n\t"
    "s_and_b32 s80, s69, 1\n\t"
    "s_add_u32 m0, s87, 0\n\t"
    "s_nop 0\n\t"
    "buffer_load_dwordx4 v20, s[20:23], s52 offen offset:0 lds\n\t"
    "s_lshl_b32 s80, s80, 11\n\t"
    "s_add_u32 s79, s79, s80\n\t"
    "s_add_u32 s79, s79, 116736\n\t"
    "v_lshlrev_b32 v6, 4, v1\n\t"
    "v_add_u32 v6, s79, v6\n\t"
    "ds_read_b128 v[44:47], v6 offset:0\n\t"
    "buffer_load_dwordx4 v21, s[20:23], s52 offen offset:1024 lds\n\t"
    "ds_read_b64 v[48:49], v6 offset:16\n\t"
    "ds_read_b128 v[60:63], v6 offset:1024\n\t"
    "ds_read_b64 v[64:65], v6 offset:1040\n\t"
    "v_add_u32 v7, 8320, v6\n\t"
    "v_add_u32 v11, 12480, v6\n\t"
    "v_add_u32 v8, 9344, v6\n\t"
    "buffer_load_dwordx4 v22, s[20:23], s52 offen offset:2048 lds\n\t"
    "v_add_u32 v12, 13504, v6\n\t"
    "s_waitcnt lgkmcnt(3)\n\t"
    "v_alignbit_b32 v52, v45, v44, 16\n\t"
    "v_alignbit_b32 v53, v46, v45, 16\n\t"
    "v_alignbit_b32 v54, v47, v46, 16\n\t"
    "s_waitcnt lgkmcnt(2)\n\t"
    "v_alignbit_b32 v55, v48, v47, 16\n\t"
    "v_alignbit_b32 v56, v49, v48, 16\n\t"
    "buffer_load_dwordx4 v23, s[20:23], s52 offen offset:3072 lds\n\t"
    "ds_write_b128 v6, v[52:55] offset:4160\n\t"
    "ds_write2_b32 v7, v45, v46 offset0:0 offset1:1\n\t"
    "ds_write2_b32 v7, v47, v48 offset0:2 offset1:3\n\t"
    "ds_write2_b32 v11, v53, v54 offset0:0 offset1:1\n\t"
    "ds_write2_b32 v11, v55, v56 offset0:2 offset1:3\n\t"
    "s_waitcnt lgkmcnt(6)\n\t"
    "v_alignbit_b32 v68, v61, v60, 16\n\t"
    "s_add_u32 m0, s87, 16384\n\t"
    "s_nop 0\n\t"
    "buffer_load_dwordx4 v20, s[20:23], s53 offen offset:0 lds\n\t"
    "v_alignbit_b32 v69, v62, v61, 16\n\t"
    "v_alignbit_b32 v70, v63, v62, 16\n\t"
    "s_waitcnt lgkmcnt(5)\n\t"
    "v_alignbit_b32 v71, v64, v63, 16\n\t"
    "v_alignbit_b32 v72, v65, v64, 16\n\t"
    "ds_write_b128 v6, v[68:71] offset:5184\n\t"
    "ds_write2_b32 v8, v61, v62 offset0:0 offset1:1\n\t"
    "buffer_load_dwordx4 v21, s[20:23], s53 offen offset:1024 lds\n\t"
    "ds_write2_b32 v8, v63, v64 offset0:2 offset1:3\n\t"
    "ds_write2_b32 v12, v69, v70 offset0:0 offset1:1\n\t"
    "ds_write2_b32 v12, v71, v72 offset0:2 offset1:3\n\t"
    "s_and_b32 s79, s69, 1\n\t"
    "s_lshl_b32 s79, s79, 11\n\t"
    "s_lshr_b32 s80, s69, 1\n\t"
    "s_mul_i32 s80, s80, 4608\n\t"
    "s_add_u32 s80, s80, 151552\n\t"
    "s_add_u32 s79, s79, s80\n\t"
    "v_lshlrev_b32 v4, 4, v1\n\t"
    "v_add_u32 v5, s79, v4\n\t"
    "v_min_u32 v6, 29, v1\n\t"
    "v_lshlrev_b32 v6, 4, v6\n\t"
    "v_add_u32 v6, s80, v6\n\t"
    "v_mov_b32 v16, 0\n\t"
    "v_mov_b32 v17, 0\n\t"
    "v_mov_b32 v18, 0\n\t"
    "v_mov_b32 v19, 0\n\t"
    "ds_write_b128 v6, v[16:19]\n\t"
    "s_waitcnt vmcnt(19)\n\t"
    "ds_write_b128 v5, v[24:27] offset:480\n\t"
    "s_waitcnt vmcnt(18)\n\t"
    "ds_write_b128 v5, v[28:31] offset:1504\n\t"
    "s_waitcnt lgkmcnt(0)\n\t"
    "s_barrier\n\t"
    "; conv main loop\n\t"
    "s_lshr_b32 s78, s69, 1\n\t"
    "s_mul_i32 s79, s78, 4608\n\t"
    "s_and_b32 s80, s69, 1\n\t"
    "s_lshl_b32 s80, s80, 8\n\t"
    "s_sub_u32 s79, s79, s80\n\t"
    "s_add_u32 s79, s79, 151808\n\t"
    "v_lshlrev_b32 v4, 4, v3\n\t"
    "v_lshl_add_u32 v4, v2, 5, v4\n\t"
    "v_add_u32 v4, s79, v4\n\t"
    "s_mul_i32 s79, s78, 16640\n\t"
    "s_sub_u32 s79, s79, s80\n\t"
    "s_add_u32 s79, s79, 129240\n\t"
    "v_and_b32 v6, 3, v1\n\t"
    "v_mul_u32_u24 v6, 4160, v6\n\t"
    "v_lshrrev_b32 v7, 2, v2\n\t"
    "v_lshlrev_b32 v7, 3, v7\n\t"
    "v_lshlrev_b32 v5, 4, v3\n\t"
    "v_sub_u32 v5, v5, v6\n\t"
    "v_sub_u32 v5, v5, v7\n\t"
    "v_add_u32 v5, s79, v5\n\t"
    "ds_read_b128 v[96:99], v4 offset:192\n\t"
    "ds_read_b64 v[64:65], v5 offset:4032\n\t"
    "ds_read_b64 v[66:67], v5 offset:4040\n\t"
    "ds_read_b128 v[100:103], v4 offset:704\n\t"
    "ds_read_b64 v[68:69], v5 offset:3520\n\t"
    "ds_read_b64 v[70:71], v5 offset:3528\n\t"
    "ds_read_b128 v[104:107], v4 offset:1216\n\t"
    "ds_read_b64 v[72:73], v5 offset:3008\n\t"
    "ds_read_b64 v[74:75], v5 offset:3016\n\t"
    "ds_read_b128 v[108:111], v4 offset:1728\n\t"
    "ds_read_b64 v[76:77], v5 offset:2496\n\t"
    "ds_read_b64 v[78:79], v5 offset:2504\n\t"
    "ds_read_b128 v[112:115], v4 offset:2240\n\t"
    "ds_read_b64 v[80:81], v5 offset:1984\n\t"
    "ds_read_b64 v[82:83], v5 offset:1992\n\t"
    "ds_read_b128 v[116:119], v4 offset:2752\n\t"
    "ds_read_b64 v[84:85], v5 offset:1472\n\t"
    "ds_read_b64 v[86:87], v5 offset:1480\n\t"
    "ds_read_b128 v[120:123], v4 offset:3264\n\t"
    "ds_read_b64 v[88:89], v5 offset:960\n\t"
    "ds_read_b64 v[90:91], v5 offset:968\n\t"
    "ds_read_b128 v[124:127], v4 offset:3776\n\t"
    "ds_read_b64 v[92:93], v5 offset:448\n\t"
    "ds_read_b64 v[94:95], v5 offset:456\n\t"
    "s_waitcnt lgkmcnt(0)\n\t"
    "v_mfma_f32_16x16x32_bf16 v[32:35], v[64:67], v[96:99], 0\n\t"
    "ds_read_b128 v[160:163], v4 offset:128\n\t"
    "v_mfma_f32_16x16x32_bf16 v[36:39], v[64:67], v[100:103], 0\n\t"
    "ds_read_b64 v[128:129], v5 offset:3968\n\t"
    "v_mfma_f32_16x16x32_bf16 v[36:39], v[68:71], v[96:99], v[36:39]\n\t"
    "ds_read_b64 v[130:131], v5 offset:3976\n\t"
    "s_waitcnt vmcnt(9)\n\t"
    "v_lshlrev_b32 v208, 16, v192\n\t"
    "v_and_b32 v209, 0xffff0000, v192\n\t"
    "v_mfma_f32_16x16x32_bf16 v[40:43], v[68:71], v[100:103], 0\n\t"
    "ds_read_b128 v[164:167], v4 offset:640\n\t"
    "v_mfma_f32_16x16x32_bf16 v[40:43], v[64:67], v[104:107], v[40:43]\n\t"
    "ds_read_b64 v[132:133], v5 offset:3456\n\t"
    "s_waitcnt vmcnt(7)\n\t"
    "v_lshlrev_b32 v212, 16, v200\n\t"
    "v_and_b32 v213, 0xffff0000, v200\n\t"
    "v_mfma_f32_16x16x32_bf16 v[40:43], v[72:75], v[96:99], v[40:43]\n\t"
    "ds_read_b64 v[134:135], v5 offset:3464\n\t"
    "v_mfma_f32_16x16x32_bf16 v[44:47], v[68:71], v[104:107], 0\n\t"
    "ds_read_b128 v[168:171], v4 offset:1152\n\t"
    "v_pk_mul_f32 v[208:209], v[208:209], v[212:213]\n\t"
    "v_cvt_pk_bf16_f32 v192, v208, v209\n\t"
    "v_mfma_f32_16x16x32_bf16 v[44:47], v[72:75], v[100:103], v[44:47]\n\t"
    "ds_read_b64 v[136:137], v5 offset:2944\n\t"
    "v_mfma_f32_16x16x32_bf16 v[48:51], v[72:75], v[104:107], 0\n\t"
    "ds_read_b64 v[138:139], v5 offset:2952\n\t"
    "s_barrier\n\t"
    "s_cmp_eq_u32 s11, 0\n\t"
    "s_cbranch_scc1 LGM_hp_14\n\t"
    "s_cmp_lg_u32 s69, 0\n\t"
    "s_cbranch_scc1 LGM_fl_15\n\t"
    "s_lshl_b32 s78, s8, 4\n\t"
    "s_add_u32 s78, s78, s9\n\t"
    "s_lshl_b32 s78, s78, 3\n\t"
    "s_add_u32 s78, s78, 8192\n\t"
    "v_mov_b32 v16, s66\n\t"
    "v_mov_b32 v17, s67\n\t"
    "v_mov_b32 v18, 0\n\t"
    "s_mov_b64 exec, 1\n\t"
    "buffer_store_dwordx2 v[16:17], v18, s[24:27], s78 offen sc1\n\t"
    "s_mov_b64 exec, -1\n\t"
    "LGM_fl_15:\n\t"
    "LGM_hp_14:\n\t"
    "v_lshlrev_b32 v210, 16, v193\n\t"
    "v_and_b32 v211, 0xffff0000, v193\n\t"
    "v_mfma_f32_16x16x32_bf16 v[44:47], v[64:67], v[108:111], v[44:47]\n\t"
    "ds_read_b128 v[172:175], v4 offset:1664\n\t"
    "v_mfma_f32_16x16x32_bf16 v[44:47], v[76:79], v[96:99], v[44:47]\n\t"
    "ds_read_b64 v[140:141], v5 offset:2432\n\t"
    "v_mfma_f32_16x16x32_bf16 v[48:51], v[68:71], v[108:111], v[48:51]\n\t"
    "ds_read_b64 v[142:143], v5 offset:2440\n\t"
    "buffer_load_dwordx4 v22, s[20:23], s53 offen offset:2048 lds\n\t"
    "v_lshlrev_b32 v214, 16, v201\n\t"
    "v_and_b32 v215, 0xffff0000, v201\n\t"
    "v_mfma_f32_16x16x32_bf16 v[48:51], v[76:79], v[100:103], v[48:51]\n\t"
    "v_mfma_f32_16x16x32_bf16 v[52:55], v[72:75], v[108:111], 0\n\t"
    "ds_read_b128 v[176:179], v4 offset:2176\n\t"
    "v_pk_mul_f32 v[210:211], v[210:211], v[214:215]\n\t"
    "v_cvt_pk_bf16_f32 v193, v210, v211\n\t"
    "v_mfma_f32_16x16x32_bf16 v[52:55], v[76:79], v[104:107], v[52:55]\n\t"
    "ds_read_b64 v[144:145], v5 offset:1920\n\t"
    "v_mfma_f32_16x16x32_bf16 v[56:59], v[76:79], v[108:111], 0\n\t"
    "ds_read_b64 v[146:147], v5 offset:1928\n\t"
    "buffer_load_dwordx4 v23, s[20:23], s53 offen offset:3072 lds\n\t"
    "v_lshlrev_b32 v208, 16, v194\n\t"
    "v_and_b32 v209, 0xffff0000, v194\n\t"
    "v_mfma_f32_16x16x32_bf16 v[48:51], v[64:67], v[112:115], v[48:51]\n\t"
    "ds_read_b128 v[180:183], v4 offset:2688\n\t"
    "v_mfma_f32_16x16x32_bf16 v[48:51], v[80:83], v[96:99], v[48:51]\n\t"
    "ds_read_b64 v[148:149], v5 offset:1408\n\t"
    "v_lshlrev_b32 v212, 16, v202\n\t"
    "v_and_b32 v213, 0xffff0000, v202\n\t"
    "v_mfma_f32_16x16x32_bf16 v[52:55], v[68:71], v[112:115], v[52:55]\n\t"
    "ds_read_b64 v[150:151], v5 offset:1416\n\t"
    "s_add_u32 m0, s87, 32768\n\t"
    "s_nop 0\n\t"
    "buffer_load_dwordx4 v20, s[20:23], s54 offen offset:0 lds\n\t"
    "v_mfma_f32_16x16x32_bf16 v[52:55], v[80:83], v[100:103], v[52:55]\n\t"
    "ds_read_b128 v[184:187], v4 offset:3200\n\t"
    "v_mfma_f32_16x16x32_bf16 v[56:59], v[72:75], v[112:115], v[56:59]\n\t"
    "ds_read_b64 v[152:153], v5 offset:896\n\t"
    "v_pk_mul_f32 v[208:209], v[208:209], v[212:213]\n\t"
    "v_cvt_pk_bf16_f32 v194, v208, v209\n\t"
    "v_mfma_f32_16x16x32_bf16 v[56:59], v[80:83], v[104:107], v[56:59]\n\t"
    "ds_read_b64 v[154:155], v5 offset:904\n\t"
    "buffer_load_dwordx4 v21, s[20:23], s54 offen offset:1024 lds\n\t"
    "v_mfma_f32_16x16x32_bf16 v[60:63], v[76:79], v[112:115], 0\n\t"
    "ds_read_b128 v[188:191], v4 offset:3712\n\t"
    "v_lshlrev_b32 v210, 16, v195\n\t"
    "v_and_b32 v211, 0xffff0000, v195\n\t"
    "v_mfma_f32_16x16x32_bf16 v[60:63], v[80:83], v[108:111], v[60:63]\n\t"
    "ds_read_b64 v[156:157], v5 offset:384\n\t"
    "v_mfma_f32_16x16x32_bf16 v[52:55], v[64:67], v[116:119], v[52:55]\n\t"
    "ds_read_b64 v[158:159], v5 offset:392\n\t"
    "v_lshlrev_b32 v214, 16, v203\n\t"
    "v_and_b32 v215, 0xffff0000, v203\n\t"
    "v_mfma_f32_16x16x32_bf16 v[52:55], v[84:87], v[96:99], v[52:55]\n\t"
    "buffer_load_dwordx4 v22, s[20:23], s54 offen offset:2048 lds\n\t"
    "v_mfma_f32_16x16x32_bf16 v[56:59], v[68:71], v[116:119], v[56:59]\n\t"
    "v_pk_mul_f32 v[210:211], v[210:211], v[214:215]\n\t"
    "v_cvt_pk_bf16_f32 v195, v210, v211\n\t"
    "v_mfma_f32_16x16x32_bf16 v[56:59], v[84:87], v[100:103], v[56:59]\n\t"
    "v_mfma_f32_16x16x32_bf16 v[60:63], v[72:75], v[116:119], v[60:63]\n\t"
    "buffer_load_dwordx4 v23, s[20:23], s54 offen offset:3072 lds\n\t"
    "v_mfma_f32_16x16x32_bf16 v[60:63], v[84:87], v[104:107], v[60:63]\n\t"
    "v_lshlrev_b32 v208, 16, v196\n\t"
    "v_and_b32 v209, 0xffff0000, v196\n\t"
    "v_mfma_f32_16x16x32_bf16 v[56:59], v[64:67], v[120:123], v[56:59]\n\t"
    "v_mfma_f32_16x16x32_bf16 v[56:59], v[88:91], v[96:99], v[56:59]\n\t"
    "s_add_u32 m0, s87, 49152\n\t"
    "s_nop 0\n\t"
    "buffer_load_dwordx4 v20, s[20:23], s55 offen offset:0 lds\n\t"
    "s_waitcnt vmcnt(13)\n\t"
    "v_lshlrev_b32 v212, 16, v204\n\t"
    "v_and_b32 v213, 0xffff0000, v204\n\t"
    "v_mfma_f32_16x16x32_bf16 v[60:63], v[68:71], v[120:123], v[60:63]\n\t"
    "v_mfma_f32_16x16x32_bf16 v[60:63], v[88:91], v[100:103], v[60:63]\n\t"
    "v_pk_mul_f32 v[208:209], v[208:209], v[212:213]\n\t"
    "v_cvt_pk_bf16_f32 v196, v208, v209\n\t"
    "v_mfma_f32_16x16x32_bf16 v[60:63], v[64:67], v[124:127], v[60:63]\n\t"
    "buffer_load_dwordx4 v21, s[20:23], s55 offen offset:1024 lds\n\t"
    "v_mfma_f32_16x16x32_bf16 v[60:63], v[92:95], v[96:99], v[60:63]\n\t"
    "v_lshlrev_b32 v210, 16, v197\n\t"
    "v_and_b32 v211, 0xffff0000, v197\n\t"
    "s_waitcnt lgkmcnt(0)\n\t"
    "v_mfma_f32_16x16x32_bf16 v[32:35], v[128:131], v[160:163], v[32:35]\n\t"
    "ds_read_b128 v[96:99], v4 offset:64\n\t"
    "v_mfma_f32_16x16x32_bf16 v[36:39], v[128:131], v[164:167], v[36:39]\n\t"
    "ds_read_b64 v[64:65], v5 offset:3904\n\t"
    "v_mfma_f32_16x16x32_bf16 v[36:39], v[132:135], v[160:163], v[36:39]\n\t"
    "ds_read_b64 v[66:67], v5 offset:3912\n\t"
    "buffer_load_dwordx4 v22, s[20:23], s55 offen offset:2048 lds\n\t"
    "v_lshlrev_b32 v214, 16, v205\n\t"
    "v_and_b32 v215, 0xffff0000, v205\n\t"
    "v_mfma_f32_16x16x32_bf16 v[40:43], v[132:135], v[164:167], v[40:43]\n\t"
    "ds_read_b128 v[100:103], v4 offset:576\n\t"
    "v_mfma_f32_16x16x32_bf16 v[40:43], v[128:131], v[168:171], v[40:43]\n\t"
    "ds_read_b64 v[68:69], v5 offset:3392\n\t"
    "v_pk_mul_f32 v[210:211], v[210:211], v[214:215]\n\t"
    "v_cvt_pk_bf16_f32 v197, v210, v211\n\t"
    "v_mfma_f32_16x16x32_bf16 v[40:43], v[136:139], v[160:163], v[40:43]\n\t"
    "ds_read_b64 v[70:71], v5 offset:3400\n\t"
    "v_mfma_f32_16x16x32_bf16 v[44:47], v[132:135], v[168:171], v[44:47]\n\t"
    "ds_read_b128 v[104:107], v4 offset:1088\n\t"
    "buffer_load_dwordx4 v23, s[20:23], s55 offen offset:3072 lds\n\t"
    "v_lshlrev_b32 v208, 16, v198\n\t"
    "v_and_b32 v209, 0xffff0000, v198\n\t"
    "v_mfma_f32_16x16x32_bf16 v[44:47], v[136:139], v[164:167], v[44:47]\n\t"
    "ds_read_b64 v[72:73], v5 offset:2880\n\t"
    "v_mfma_f32_16x16x32_bf16 v[48:51], v[136:139], v[168:171], v[48:51]\n\t"
    "ds_read_b64 v[74:75], v5 offset:2888\n\t"
    "v_lshlrev_b32 v212, 16, v206\n\t"
    "v_and_b32 v213, 0xffff0000, v206\n\t"
    "v_mfma_f32_16x16x32_bf16 v[44:47], v[128:131], v[172:175], v[44:47]\n\t"
    "ds_read_b128 v[108:111], v4 offset:1600\n\t"
    "v_mfma_f32_16x16x32_bf16 v[44:47], v[140:143], v[160:163], v[44:47]\n\t"
    "ds_read_b64 v[76:77], v5 offset:2368\n\t"
    "s_add_u32 m0, s87, 65536\n\t"
    "s_nop 0\n\t"
    "buffer_load_dwordx4 v20, s[20:23], s56 offen offset:0 lds\n\t"
    "v_mfma_f32_16x16x32_bf16 v[48:51], v[132:135], v[172:175], v[48:51]\n\t"
    "ds_read_b64 v[78:79], v5 offset:2376\n\t"
    "v_pk_mul_f32 v[208:209], v[208:209], v[212:213]\n\t"
    "v_cvt_pk_bf16_f32 v198, v208, v209\n\t"
    "v_mfma_f32_16x16x32_bf16 v[48:51], v[140:143], v[164:167], v[48:51]\n\t"
    "v_mfma_f32_16x16x32_bf16 v[52:55], v[136:139], v[172:175], v[52:55]\n\t"
    "ds_read_b128 v[112:115], v4 offset:2112\n\t"
    "v_lshlrev_b32 v210, 16, v199\n\t"
    "v_and_b32 v211, 0xffff0000, v199\n\t"
    "v_mfma_f32_16x16x32_bf16 v[52:55], v[140:143], v[168:171], v[52:55]\n\t"
    "ds_read_b64 v[80:81], v5 offset:1856\n\t"
    "buffer_load_dwordx4 v21, s[20:23], s56 offen offset:1024 lds\n\t"
    "v_mfma_f32_16x16x32_bf16 v[56:59], v[140:143], v[172:175], v[56:59]\n\t"
    "ds_read_b64 v[82:83], v5 offset:1864\n\t"
    "v_lshlrev_b32 v214, 16, v207\n\t"
    "v_and_b32 v215, 0xffff0000, v207\n\t"
    "v_mfma_f32_16x16x32_bf16 v[48:51], v[128:131], v[176:179], v[48:51]\n\t"
    "ds_read_b128 v[116:119], v4 offset:2624\n\t"
    "v_mfma_f32_16x16x32_bf16 v[48:51], v[144:147], v[160:163], v[48:51]\n\t"
    "ds_read_b64 v[84:85], v5 offset:1344\n\t"
    "v_pk_mul_f32 v[210:211], v[210:211], v[214:215]\n\t"
    "v_cvt_pk_bf16_f32 v199, v210, v211\n\t"
    "v_mfma_f32_16x16x32_bf16 v[52:55], v[132:135], v[176:179], v[52:55]\n\t"
    "ds_read_b64 v[86:87], v5 offset:1352\n\t"
    "buffer_load_dwordx4 v22, s[20:23], s56 offen offset:2048 lds\n\t"
    "v_mfma_f32_16x16x32_bf16 v[52:55], v[144:147], v[164:167], v[52:55]\n\t"
    "ds_read_b128 v[120:123], v4 offset:3136\n\t"
    "v_mfma_f32_16x16x32_bf16 v[56:59], v[136:139], v[176:179], v[56:59]\n\t"
    "ds_read_b64 v[88:89], v5 offset:832\n\t"
    "v_lshlrev_b32 v212, 16, v192\n\t"
    "v_and_b32 v213, 0xffff0000, v192\n\t"
    "v_mfma_f32_16x16x32_bf16 v[56:59], v[144:147], v[168:171], v[56:59]\n\t"
    "ds_read_b64 v[90:91], v5 offset:840\n\t"
    "v_mfma_f32_16x16x32_bf16 v[60:63], v[140:143], v[176:179], v[60:63]\n\t"
    "ds_read_b128 v[124:127], v4 offset:3648\n\t"
    "buffer_load_dwordx4 v23, s[20:23], s56 offen offset:3072 lds\n\t"
    "v_lshlrev_b32 v210, 16, v193\n\t"
    "v_and_b32 v211, 0xffff0000, v193\n\t"
    "v_mfma_f32_16x16x32_bf16 v[60:63], v[144:147], v[172:175], v[60:63]\n\t"
    "ds_read_b64 v[92:93], v5 offset:320\n\t"
    "v_mfma_f32_16x16x32_bf16 v[52:55], v[128:131], v[180:183], v[52:55]\n\t"
    "ds_read_b64 v[94:95], v5 offset:328\n\t"
    "v_pk_add_f32 v[212:213], v[212:213], v[210:211]\n\t"
    "v_lshlrev_b32 v208, 16, v194\n\t"
    "v_mfma_f32_16x16x32_bf16 v[52:55], v[148:151], v[160:163], v[52:55]\n\t"
    "v_mfma_f32_16x16x32_bf16 v[56:59], v[132:135], v[180:183], v[56:59]\n\t"
    "s_add_u32 m0, s87, 81920\n\t"
    "s_nop 0\n\t"
    "buffer_load_dwordx4 v20, s[20:23], s57 offen offset:0 lds\n\t"
    "v_and_b32 v209, 0xffff0000, v194\n\t"
    "v_pk_add_f32 v[212:213], v[212:213], v[208:209]\n\t"
    "v_mfma_f32_16x16x32_bf16 v[56:59], v[148:151], v[164:167], v[56:59]\n\t"
    "v_mfma_f32_16x16x32_bf16 v[60:63], v[136:139], v[180:183], v[60:63]\n\t"
    "v_mfma_f32_16x16x32_bf16 v[60:63], v[148:151], v[168:171], v[60:63]\n\t"
    "v_lshlrev_b32 v210, 16, v195\n\t"
    "v_and_b32 v211, 0xffff0000, v195\n\t"
    "v_mfma_f32_16x16x32_bf16 v[56:59], v[128:131], v[184:187], v[56:59]\n\t"
    "buffer_load_dwordx4 v21, s[20:23], s57 offen offset:1024 lds\n\t"
    "v_mfma_f32_16x16x32_bf16 v[56:59], v[152:155], v[160:163], v[56:59]\n\t"
    "v_pk_add_f32 v[212:213], v[212:213], v[210:211]\n\t"
    "v_lshlrev_b32 v208, 16, v196\n\t"
    "v_mfma_f32_16x16x32_bf16 v[60:63], v[132:135], v[184:187], v[60:63]\n\t"
    "v_mfma_f32_16x16x32_bf16 v[60:63], v[152:155], v[164:167], v[60:63]\n\t"
    "v_and_b32 v209, 0xffff0000, v196\n\t"
    "v_pk_add_f32 v[212:213], v[212:213], v[208:209]\n\t"
    "v_mfma_f32_16x16x32_bf16 v[60:63], v[128:131], v[188:191], v[60:63]\n\t"
    "s_lshr_b32 s78, s72, 1\n\t"
    "s_lshl_b32 s78, s78, 7\n\t"
    "s_add_u32 s78, s78, 8192\n\t"
    "v_min_u32 v13, 13, v2\n\t"
    "v_lshlrev_b32 v13, 3, v13\n\t"
    "v_add_u32 v13, s78, v13\n\t"
    "buffer_load_dwordx2 v[14:15], v13, s[24:27], 0 offen sc1\n\t"
    "v_mfma_f32_16x16x32_bf16 v[60:63], v[156:159], v[160:163], v[60:63]\n\t"
    "v_lshlrev_b32 v210, 16, v197\n\t"
    "v_and_b32 v211, 0xffff0000, v197\n\t"
    "s_waitcnt lgkmcnt(0)\n\t"
    "v_mfma_f32_16x16x32_bf16 v[32:35], v[64:67], v[96:99], v[32:35]\n\t"
    "ds_read_b128 v[160:163], v4 offset:0\n\t"
    "v_mfma_f32_16x16x32_bf16 v[36:39], v[64:67], v[100:103], v[36:39]\n\t"
    "ds_read_b64 v[128:129], v5 offset:3840\n\t"
    "v_mfma_f32_16x16x32_bf16 v[36:39], v[68:71], v[96:99], v[36:39]\n\t"
    "ds_read_b64 v[130:131], v5 offset:3848\n\t"
    "buffer_load_dwordx4 v22, s[20:23], s57 offen offset:2048 lds\n\t"
    "v_pk_add_f32 v[212:213], v[212:213], v[210:211]\n\t"
    "v_lshlrev_b32 v208, 16, v198\n\t"
    "v_mfma_f32_16x16x32_bf16 v[40:43], v[68:71], v[100:103], v[40:43]\n\t"
    "ds_read_b128 v[164:167], v4 offset:512\n\t"
    "v_mfma_f32_16x16x32_bf16 v[40:43], v[64:67], v[104:107], v[40:43]\n\t"
    "ds_read_b64 v[132:133], v5 offset:3328\n\t"
    "v_and_b32 v209, 0xffff0000, v198\n\t"
    "v_pk_add_f32 v[212:213], v[212:213], v[208:209]\n\t"
    "v_mfma_f32_16x16x32_bf16 v[40:43], v[72:75], v[96:99], v[40:43]\n\t"
    "ds_read_b64 v[134:135], v5 offset:3336\n\t"
    "buffer_load_dwordx4 v23, s[20:23], s57 offen offset:3072 lds\n\t"
    "v_mfma_f32_16x16x32_bf16 v[44:47], v[68:71], v[104:107], v[44:47]\n\t"
    "ds_read_b128 v[168:171], v4 offset:1024\n\t"
    "v_mfma_f32_16x16x32_bf16 v[44:47], v[72:75], v[100:103], v[44:47]\n\t"
    "ds_read_b64 v[136:137], v5 offset:2816\n\t"
    "v_lshlrev_b32 v210, 16, v199\n\t"
    "v_and_b32 v211, 0xffff0000, v199\n\t"
    "v_mfma_f32_16x16x32_bf16 v[48:51], v[72:75], v[104:107], v[48:51]\n\t"
    "ds_read_b64 v[138:139], v5 offset:2824\n\t"
    "v_mfma_f32_16x16x32_bf16 v[44:47], v[64:67], v[108:111], v[44:47]\n\t"
    "ds_read_b128 v[172:175], v4 offset:1536\n\t"
    "s_add_u32 m0, s87, 98304\n\t"
    "s_nop 0\n\t"
    "buffer_load_dwordx4 v20, s[20:23], s58 offen offset:0 lds\n\t"
    "v_pk_add_f32 v[212:213], v[212:213], v[210:211]\n\t"
    "v_add_f32 v216, v212, v213\n\t"
    "v_mfma_f32_16x16x32_bf16 v[44:47], v[76:79], v[96:99], v[44:47]\n\t"
    "ds_read_b64 v[140:141], v5 offset:2304\n\t"
    "v_mfma_f32_16x16x32_bf16 v[48:51], v[68:71], v[108:111], v[48:51]\n\t"
    "ds_read_b64 v[142:143], v5 offset:2312\n\t"
    "v_add_f32_dpp v216, v216, v216 row_shr:1 bound_ctrl:0\n\t"
    "s_nop 1\n\t"
    "v_add_f32_dpp v216, v216, v216 row_shr:2 bound_ctrl:0\n\t"
    "v_mfma_f32_16x16x32_bf16 v[48:51], v[76:79], v[100:103], v[48:51]\n\t"
    "v_mfma_f32_16x16x32_bf16 v[52:55], v[72:75], v[108:111], v[52:55]\n\t"
    "ds_read_b128 v[176:179], v4 offset:2048\n\t"
    "buffer_load_dwordx4 v21, s[20:23], s58 offen offset:1024 lds\n\t"
    "v_mfma_f32_16x16x32_bf16 v[52:55], v[76:79], v[104:107], v[52:55]\n\t"
    "ds_read_b64 v[144:145], v5 offset:1792\n\t"
    "v_add_f32_dpp v216, v216, v216 row_shr:4 bound_ctrl:0\n\t"
    "s_nop 1\n\t"
    "v_add_f32_dpp v216, v216, v216 row_shr:8 bound_ctrl:0\n\t"
    "v_mfma_f32_16x16x32_bf16 v[56:59], v[76:79], v[108:111], v[56:59]\n\t"
    "ds_read_b64 v[146:147], v5 offset:1800\n\t"
    "v_mfma_f32_16x16x32_bf16 v[48:51], v[64:67], v[112:115], v[48:51]\n\t"
    "ds_read_b128 v[180:183], v4 offset:2560\n\t"
    "buffer_load_dwordx4 v22, s[20:23], s58 offen offset:2048 lds\n\t"
    "v_add_f32_dpp v216, v216, v216 row_bcast:15 row_mask:0xa\n\t"
    "v_cvt_pk_bf16_f32 v216, v216, v216\n\t"
    "v_mfma_f32_16x16x32_bf16 v[48:51], v[80:83], v[96:99], v[48:51]\n\t"
    "ds_read_b64 v[148:149], v5 offset:1280\n\t"
    "v_mfma_f32_16x16x32_bf16 v[52:55], v[68:71], v[112:115], v[52:55]\n\t"
    "ds_read_b64 v[150:151], v5 offset:1288\n\t"
    "v_mfma_f32_16x16x32_bf16 v[52:55], v[80:83], v[100:103], v[52:55]\n\t"
    "ds_read_b128 v[184:187], v4 offset:3072\n\t"
    "v_lshlrev_b32 v216, 16, v216\n\t"
    "v_mul_f32 v216, 0xbfb8aa3b, v216\n\t"
    "v_mfma_f32_16x16x32_bf16 v[56:59], v[72:75], v[112:115], v[56:59]\n\t"
    "ds_read_b64 v[152:153], v5 offset:768\n\t"
    "buffer_load_dwordx4 v23, s[20:23], s58 offen offset:3072 lds\n\t"
    "v_mfma_f32_16x16x32_bf16 v[56:59], v[80:83], v[104:107], v[56:59]\n\t"
    "ds_read_b64 v[154:155], v5 offset:776\n\t"
    "v_exp_f32 v216, v216\n\t"
    "v_lshrrev_b32 v208, 5, v1\n\t"
    "v_mfma_f32_16x16x32_bf16 v[60:63], v[76:79], v[112:115], v[60:63]\n\t"
    "ds_read_b128 v[188:191], v4 offset:3584\n\t"
    "v_mfma_f32_16x16x32_bf16 v[60:63], v[80:83], v[108:111], v[60:63]\n\t"
    "ds_read_b64 v[156:157], v5 offset:256\n\t"
    "s_waitcnt vmcnt(6)\n\t"
    "v_cmp_eq_u64 vcc, s[66:67], v[14:15]\n\t"
    "s_nop 1\n\t"
    "s_cmp_eq_u64 vcc, -1\n\t"
    "s_cbranch_scc1 LGM_p2ok_16\n\t"
    "s_mov_b32 s77, 0\n\t"
    "LGM_p2_spin_17:\n\t"
    "buffer_load_dwordx2 v[14:15], v13, s[24:27], 0 offen sc1\n\t"
    "s_waitcnt vmcnt(0)\n\t"
    "v_cmp_eq_u64 vcc, s[66:67], v[14:15]\n\t"
    "s_nop 1\n\t"
    "s_cmp_eq_u64 vcc, -1\n\t"
    "s_cbranch_scc1 LGM_p2_done_18\n\t"
    "s_add_u32 s77, s77, 1\n\t"
    "s_cmp_gt_u32 s77, 20000\n\t"
    "s_cbranch_scc1 LGM_p2_done_18\n\t"
    "s_sleep 1\n\t"
    "s_branch LGM_p2_spin_17\n\t"
    "LGM_p2_done_18:\n\t"
    "LGM_p2ok_16:\n\t"
    "s_add_u32 s79, s69, 0\n\t"
    "s_and_b32 s79, s79, 3\n\t"
    "s_mul_i32 s79, s79, 57344\n\t"
    "v_lshlrev_b32 v24, 4, v1\n\t"
    "v_add_u32 v24, s79, v24\n\t"
    "s_add_u32 s79, s69, 1\n\t"
    "s_and_b32 s79, s79, 3\n\t"
    "s_mul_i32 s79, s79, 57344\n\t"
    "v_lshlrev_b32 v25, 4, v1\n\t"
    "v_add_u32 v25, s79, v25\n\t"
    "s_add_u32 s79, s69, 2\n\t"
    "s_and_b32 s79, s79, 3\n\t"
    "s_mul_i32 s79, s79, 57344\n\t"
    "v_lshlrev_b32 v26, 4, v1\n\t"
    "v_add_u32 v26, s79, v26\n\t"
    "s_add_u32 s79, s69, 3\n\t"
    "s_and_b32 s79, s79, 3\n\t"
    "s_mul_i32 s79, s79, 57344\n\t"
    "v_lshlrev_b32 v27, 4, v1\n\t"
    "v_add_u32 v27, s79, v27\n\t"
    "s_mul_i32 s90, s72, 229376\n\t"
    "s_add_u32 s90, s90, 4259840\n\t"
    "v_add_f32 v216, 1.0, v216\n\t"
    "v_rcp_f32 v216, v216\n\t"
    "v_mfma_f32_16x16x32_bf16 v[52:55], v[64:67], v[116:119], v[52:55]\n\t"
    "ds_read_b64 v[158:159], v5 offset:264\n\t"
    "v_mfma_f32_16x16x32_bf16 v[52:55], v[84:87], v[96:99], v[52:55]\n\t"
    "v_mfma_f32_16x16x32_bf16 v[56:59], v[68:71], v[116:119], v[56:59]\n\t"
    "v_lshlrev_b32 v208, 2, v208\n\t"
    "s_lshl_b32 s94, s70, 7\n\t"
    "v_mfma_f32_16x16x32_bf16 v[56:59], v[84:87], v[100:103], v[56:59]\n\t"
    "buffer_load_dwordx4 a[0:3], v24, s[24:27], s36 offen sc1\n\t"
    "v_mfma_f32_16x16x32_bf16 v[60:63], v[72:75], v[116:119], v[60:63]\n\t"
    "s_lshl_b32 s92, s71, 3\n\t"
    "v_cvt_pk_bf16_f32 v216, v216, v216\n\t"
    "v_mfma_f32_16x16x32_bf16 v[60:63], v[84:87], v[104:107], v[60:63]\n\t"
    "v_mfma_f32_16x16x32_bf16 v[56:59], v[64:67], v[120:123], v[56:59]\n\t"
    "v_mfma_f32_16x16x32_bf16 v[56:59], v[88:91], v[96:99], v[56:59]\n\t"
    "buffer_load_dwordx4 a[4:7], v25, s[24:27], s36 offen sc1\n\t"
    "s_add_u32 s94, s94, s92\n\t"
    "s_lshl_b32 s92, s69, 1\n\t"
    "v_mfma_f32_16x16x32_bf16 v[60:63], v[68:71], v[120:123], v[60:63]\n\t"
    "v_mfma_f32_16x16x32_bf16 v[60:63], v[88:91], v[100:103], v[60:63]\n\t"
    "v_lshlrev_b32 v216, 16, v216\n\t"
    "s_add_u32 s94, s94, s92\n\t"
    "v_mfma_f32_16x16x32_bf16 v[60:63], v[64:67], v[124:127], v[60:63]\n\t"
    "buffer_load_dwordx4 a[8:11], v26, s[24:27], s36 offen sc1\n\t"
    "v_mfma_f32_16x16x32_bf16 v[60:63], v[92:95], v[96:99], v[60:63]\n\t"
    "s_lshl_b32 s94, s94, 2\n\t"
    "s_add_u32 s94, s94, 16384\n\t"
    "s_waitcnt lgkmcnt(0)\n\t"
    "v_mfma_f32_16x16x32_bf16 v[32:35], v[128:131], v[160:163], v[32:35]\n\t"
    "v_mfma_f32_16x16x32_bf16 v[36:39], v[128:131], v[164:167], v[36:39]\n\t"
    "v_mfma_f32_16x16x32_bf16 v[36:39], v[132:135], v[160:163], v[36:39]\n\t"
    "s_mov_b32 exec_lo, 0x80000000\n\t"
    "s_mov_b32 exec_hi, 0x80000000\n\t"
    "buffer_store_dword v216, v208, s[24:27], s94 offen sc1\n\t"
    "s_mov_b64 exec, -1\n\t"
    "v_mfma_f32_16x16x32_bf16 v[40:43], v[132:135], v[164:167], v[40:43]\n\t"
    "v_mfma_f32_16x16x32_bf16 v[40:43], v[128:131], v[168:171], v[40:43]\n\t"
    "v_mfma_f32_16x16x32_bf16 v[40:43], v[136:139], v[160:163], v[40:43]\n\t"
    "buffer_load_dwordx4 a[12:15], v27, s[24:27], s36 offen sc1\n\t"
    "v_mfma_f32_16x16x32_bf16 v[44:47], v[132:135], v[168:171], v[44:47]\n\t"
    "v_mfma_f32_16x16x32_bf16 v[44:47], v[136:139], v[164:167], v[44:47]\n\t"
    "v_mfma_f32_16x16x32_bf16 v[48:51], v[136:139], v[168:171], v[48:51]\n\t"
    "v_mfma_f32_16x16x32_bf16 v[44:47], v[128:131], v[172:175], v[44:47]\n\t"
    "buffer_load_dwordx4 a[16:19], v24, s[24:27], s37 offen sc1\n\t"
    "v_mfma_f32_16x16x32_bf16 v[44:47], v[140:143], v[160:163], v[44:47]\n\t"
    "v_mfma_f32_16x16x32_bf16 v[48:51], v[132:135], v[172:175], v[48:51]\n\t"
    "v_mfma_f32_16x16x32_bf16 v[48:51], v[140:143], v[164:167], v[48:51]\n\t"
    "v_mfma_f32_16x16x32_bf16 v[52:55], v[136:139], v[172:175], v[52:55]\n\t"
    "buffer_load_dwordx4 a[20:23], v25, s[24:27], s37 offen sc1\n\t"
    "v_mfma_f32_16x16x32_bf16 v[52:55], v[140:143], v[168:171], v[52:55]\n\t"
    "v_mfma_f32_16x16x32_bf16 v[56:59], v[140:143], v[172:175], v[56:59]\n\t"
    "v_mfma_f32_16x16x32_bf16 v[48:51], v[128:131], v[176:179], v[48:51]\n\t"
    "buffer_load_dwordx4 a[24:27], v26, s[24:27], s37 offen sc1\n\t"
    "v_mfma_f32_16x16x32_bf16 v[48:51], v[144:147], v[160:163], v[48:51]\n\t"
    "v_mfma_f32_16x16x32_bf16 v[52:55], v[132:135], v[176:179], v[52:55]\n\t"
    "v_mfma_f32_16x16x32_bf16 v[52:55], v[144:147], v[164:167], v[52:55]\n\t"
    "v_mfma_f32_16x16x32_bf16 v[56:59], v[136:139], v[176:179], v[56:59]\n\t"
    "buffer_load_dwordx4 a[28:31], v27, s[24:27], s37 offen sc1\n\t"
    "v_mfma_f32_16x16x32_bf16 v[56:59], v[144:147], v[168:171], v[56:59]\n\t"
    "v_mfma_f32_16x16x32_bf16 v[60:63], v[140:143], v[176:179], v[60:63]\n\t"
    "v_mfma_f32_16x16x32_bf16 v[60:63], v[144:147], v[172:175], v[60:63]\n\t"
    "buffer_load_dwordx4 a[32:35], v24, s[24:27], s38 offen sc1\n\t"
    "v_mfma_f32_16x16x32_bf16 v[52:55], v[128:131], v[180:183], v[52:55]\n\t"
    "v_mfma_f32_16x16x32_bf16 v[52:55], v[148:151], v[160:163], v[52:55]\n\t"
    "v_mfma_f32_16x16x32_bf16 v[56:59], v[132:135], v[180:183], v[56:59]\n\t"
    "v_mfma_f32_16x16x32_bf16 v[56:59], v[148:151], v[164:167], v[56:59]\n\t"
    "buffer_load_dwordx4 a[36:39], v25, s[24:27], s38 offen sc1\n\t"
    "v_mfma_f32_16x16x32_bf16 v[60:63], v[136:139], v[180:183], v[60:63]\n\t"
    "v_mfma_f32_16x16x32_bf16 v[60:63], v[148:151], v[168:171], v[60:63]\n\t"
    "v_mfma_f32_16x16x32_bf16 v[56:59], v[128:131], v[184:187], v[56:59]\n\t"
    "v_mfma_f32_16x16x32_bf16 v[56:59], v[152:155], v[160:163], v[56:59]\n\t"
    "buffer_load_dwordx4 a[40:43], v26, s[24:27], s38 offen sc1\n\t"
    "v_mfma_f32_16x16x32_bf16 v[60:63], v[132:135], v[184:187], v[60:63]\n\t"
    "v_mfma_f32_16x16x32_bf16 v[60:63], v[152:155], v[164:167], v[60:63]\n\t"
    "v_mfma_f32_16x16x32_bf16 v[60:63], v[128:131], v[188:191], v[60:63]\n\t"
    "buffer_load_dwordx4 a[44:47], v27, s[24:27], s38 offen sc1\n\t"
    "v_mfma_f32_16x16x32_bf16 v[60:63], v[156:159], v[160:163], v[60:63]\n\t"
    "v_lshrrev_b32 v10, 2, v2\n\t"
    "v_lshl_add_u32 v11, v3, 2, v10\n\t"
    "v_lshrrev_b32 v12, 1, v11\n\t"
    "v_and_b32 v12, 3, v12\n\t"
    "v_lshlrev_b32 v11, 7, v11\n\t"
    "v_and_b32 v10, 1, v1\n\t"
    "v_lshl_add_u32 v11, v10, 3, v11\n\t"
    "v_bfe_u32 v10, v1, 1, 1\n\t"
    "v_lshl_add_u32 v11, v10, 4, v11\n\t"
    "v_add_u32 v11, s87, v11\n\t"
    "v_xor_b32 v10, 0, v12\n\t"
    "v_lshl_add_u32 v136, v10, 5, v11\n\t"
    "v_add_u32 v140, 65536, v136\n\t"
    "v_xor_b32 v10, 1, v12\n\t"
    "v_lshl_add_u32 v137, v10, 5, v11\n\t"
    "v_add_u32 v141, 65536, v137\n\t"
    "v_xor_b32 v10, 2, v12\n\t"
    "v_lshl_add_u32 v138, v10, 5, v11\n\t"
    "v_add_u32 v142, 65536, v138\n\t"
    "v_xor_b32 v10, 3, v12\n\t"
    "v_lshl_add_u32 v139, v10, 5, v11\n\t"
    "v_add_u32 v143, 65536, v139\n\t"
    "; conv epilogue\n\t"
    "v_lshlrev_b32 v4, 4, v1\n\t"
    "s_lshl_b32 s78, s69, 12\n\t"
    "s_add_u32 s78, s78, 116736\n\t"
    "s_xor_b32 s79, s69, 1\n\t"
    "s_lshl_b32 s79, s79, 12\n\t"
    "s_add_u32 s79, s79, 116736\n\t"
    "v_add_u32 v5, s79, v4\n\t"
    "v_add_u32 v4, s78, v4\n\t"
    "v_lshlrev_b32 v7, 3, v3\n\t"
    "v_lshl_add_u32 v7, v2, 5, v7\n\t"
    "s_lshr_b32 s80, s69, 1\n\t"
    "s_mul_i32 s80, s80, 4608\n\t"
    "s_add_u32 s80, s80, 152032\n\t"
    "v_add_u32 v6, s80, v7\n\t"
    "s_lshl_b32 s81, s74, 12\n\t"
    "s_add_u32 s81, s81, 2162688\n\t"
    "v_mov_b32 v8, s76\n\t"
    "v_mov_b32 v9, s76\n\t"
    "buffer_load_dwordx4 a[48:51], v24, s[24:27], s39 offen sc1\n\t"
    "buffer_load_dwordx4 a[52:55], v25, s[24:27], s39 offen sc1\n\t"
    "s_barrier\n\t"
    "s_and_b32 s78, s69, 1\n\t"
    "s_cmp_eq_u32 s78, 1\n\t"
    "s_cbranch_scc1 LGM_c1_19\n\t"
    "ds_write_b128 v4, v[48:51] offset:0\n\t"
    "ds_write_b128 v4, v[52:55] offset:1024\n\t"
    "ds_write_b128 v4, v[56:59] offset:2048\n\t"
    "ds_write_b128 v4, v[60:63] offset:3072\n\t"
    "ds_read_b64 v[80:81], v6 offset:0\n\t"
    "ds_read_b64 v[82:83], v6 offset:512\n\t"
    "ds_read_b64 v[84:85], v6 offset:1024\n\t"
    "ds_read_b64 v[86:87], v6 offset:1536\n\t"
    "buffer_load_dwordx4 a[56:59], v26, s[24:27], s39 offen sc1\n\t"
    "buffer_load_dwordx4 a[60:63], v27, s[24:27], s39 offen sc1\n\t"
    "s_waitcnt lgkmcnt(0)\n\t"
    "s_barrier\n\t"
    "ds_read_b128 v[64:67], v5 offset:0\n\t"
    "ds_read_b128 v[68:71], v5 offset:1024\n\t"
    "ds_read_b128 v[72:75], v5 offset:2048\n\t"
    "ds_read_b128 v[76:79], v5 offset:3072\n\t"
    "v_lshlrev_b32 v96, 16, v80\n\t"
    "v_and_b32 v97, 0xffff0000, v80\n\t"
    "v_pk_fma_f32 v[32:33], v[96:97], v[8:9], v[32:33]\n\t"
    "v_lshlrev_b32 v98, 16, v81\n\t"
    "v_and_b32 v99, 0xffff0000, v81\n\t"
    "v_pk_fma_f32 v[34:35], v[98:99], v[8:9], v[34:35]\n\t"
    "s_waitcnt lgkmcnt(3)\n\t"
    "v_pk_add_f32 v[32:33], v[32:33], v[64:65]\n\t"
    "v_pk_add_f32 v[34:35], v[34:35], v[66:67]\n\t"
    "v_cvt_pk_bf16_f32 v100, v32, v33\n\t"
    "v_cvt_pk_bf16_f32 v101, v34, v35\n\t"
    "buffer_store_dwordx2 v[100:101], v7, s[24:27], s81 offen offset:0 sc1\n\t"
    "v_lshlrev_b32 v104, 16, v82\n\t"
    "v_and_b32 v105, 0xffff0000, v82\n\t"
    "v_pk_fma_f32 v[36:37], v[104:105], v[8:9], v[36:37]\n\t"
    "v_lshlrev_b32 v106, 16, v83\n\t"
    "v_and_b32 v107, 0xffff0000, v83\n\t"
    "v_pk_fma_f32 v[38:39], v[106:107], v[8:9], v[38:39]\n\t"
    "s_waitcnt lgkmcnt(2)\n\t"
    "v_pk_add_f32 v[36:37], v[36:37], v[68:69]\n\t"
    "v_pk_add_f32 v[38:39], v[38:39], v[70:71]\n\t"
    "v_cvt_pk_bf16_f32 v108, v36, v37\n\t"
    "v_cvt_pk_bf16_f32 v109, v38, v39\n\t"
    "buffer_store_dwordx2 v[108:109], v7, s[24:27], s81 offen offset:512 sc1\n\t"
    "v_lshlrev_b32 v96, 16, v84\n\t"
    "v_and_b32 v97, 0xffff0000, v84\n\t"
    "v_pk_fma_f32 v[40:41], v[96:97], v[8:9], v[40:41]\n\t"
    "v_lshlrev_b32 v98, 16, v85\n\t"
    "v_and_b32 v99, 0xffff0000, v85\n\t"
    "v_pk_fma_f32 v[42:43], v[98:99], v[8:9], v[42:43]\n\t"
    "s_waitcnt lgkmcnt(1)\n\t"
    "v_pk_add_f32 v[40:41], v[40:41], v[72:73]\n\t"
    "v_pk_add_f32 v[42:43], v[42:43], v[74:75]\n\t"
    "v_cvt_pk_bf16_f32 v100, v40, v41\n\t"
    "v_cvt_pk_bf16_f32 v101, v42, v43\n\t"
    "buffer_store_dwordx2 v[100:101], v7, s[24:27], s81 offen offset:1024 sc1\n\t"
    "v_lshlrev_b32 v104, 16, v86\n\t"
    "v_and_b32 v105, 0xffff0000, v86\n\t"
    "v_pk_fma_f32 v[44:45], v[104:105], v[8:9], v[44:45]\n\t"
    "v_lshlrev_b32 v106, 16, v87\n\t"
    "v_and_b32 v107, 0xffff0000, v87\n\t"
    "v_pk_fma_f32 v[46:47], v[106:107], v[8:9], v[46:47]\n\t"
    "s_waitcnt lgkmcnt(0)\n\t"
    "v_pk_add_f32 v[44:45], v[44:45], v[76:77]\n\t"
    "v_pk_add_f32 v[46:47], v[46:47], v[78:79]\n\t"
    "v_cvt_pk_bf16_f32 v108, v44, v45\n\t"
    "v_cvt_pk_bf16_f32 v109, v46, v47\n\t"
    "buffer_store_dwordx2 v[108:109], v7, s[24:27], s81 offen offset:1536 sc1\n\t"
    "buffer_load_dwordx4 a[64:67], v24, s[24:27], s40 offen sc1\n\t"
    "buffer_load_dwordx4 a[68:71], v25, s[24:27], s40 offen sc1\n\t"
    "s_branch LGM_cj_20\n\t"
    "LGM_c1_19:\n\t"
    "ds_write_b128 v4, v[32:35] offset:0\n\t"
    "ds_write_b128 v4, v[36:39] offset:1024\n\t"
    "ds_write_b128 v4, v[40:43] offset:2048\n\t"
    "ds_write_b128 v4, v[44:47] offset:3072\n\t"
    "ds_read_b64 v[80:81], v6 offset:2048\n\t"
    "ds_read_b64 v[82:83], v6 offset:2560\n\t"
    "ds_read_b64 v[84:85], v6 offset:3072\n\t"
    "ds_read_b64 v[86:87], v6 offset:3584\n\t"
    "buffer_load_dwordx4 a[56:59], v26, s[24:27], s39 offen sc1\n\t"
    "buffer_load_dwordx4 a[60:63], v27, s[24:27], s39 offen sc1\n\t"
    "s_waitcnt lgkmcnt(0)\n\t"
    "s_barrier\n\t"
    "ds_read_b128 v[64:67], v5 offset:0\n\t"
    "ds_read_b128 v[68:71], v5 offset:1024\n\t"
    "ds_read_b128 v[72:75], v5 offset:2048\n\t"
    "ds_read_b128 v[76:79], v5 offset:3072\n\t"
    "v_lshlrev_b32 v96, 16, v80\n\t"
    "v_and_b32 v97, 0xffff0000, v80\n\t"
    "v_pk_fma_f32 v[48:49], v[96:97], v[8:9], v[48:49]\n\t"
    "v_lshlrev_b32 v98, 16, v81\n\t"
    "v_and_b32 v99, 0xffff0000, v81\n\t"
    "v_pk_fma_f32 v[50:51], v[98:99], v[8:9], v[50:51]\n\t"
    "s_waitcnt lgkmcnt(3)\n\t"
    "v_pk_add_f32 v[48:49], v[48:49], v[64:65]\n\t"
    "v_pk_add_f32 v[50:51], v[50:51], v[66:67]\n\t"
    "v_cvt_pk_bf16_f32 v100, v48, v49\n\t"
    "v_cvt_pk_bf16_f32 v101, v50, v51\n\t"
    "buffer_store_dwordx2 v[100:101], v7, s[24:27], s81 offen offset:2048 sc1\n\t"
    "v_lshlrev_b32 v104, 16, v82\n\t"
    "v_and_b32 v105, 0xffff0000, v82\n\t"
    "v_pk_fma_f32 v[52:53], v[104:105], v[8:9], v[52:53]\n\t"
    "v_lshlrev_b32 v106, 16, v83\n\t"
    "v_and_b32 v107, 0xffff0000, v83\n\t"
    "v_pk_fma_f32 v[54:55], v[106:107], v[8:9], v[54:55]\n\t"
    "s_waitcnt lgkmcnt(2)\n\t"
    "v_pk_add_f32 v[52:53], v[52:53], v[68:69]\n\t"
    "v_pk_add_f32 v[54:55], v[54:55], v[70:71]\n\t"
    "v_cvt_pk_bf16_f32 v108, v52, v53\n\t"
    "v_cvt_pk_bf16_f32 v109, v54, v55\n\t"
    "buffer_store_dwordx2 v[108:109], v7, s[24:27], s81 offen offset:2560 sc1\n\t"
    "v_lshlrev_b32 v96, 16, v84\n\t"
    "v_and_b32 v97, 0xffff0000, v84\n\t"
    "v_pk_fma_f32 v[56:57], v[96:97], v[8:9], v[56:57]\n\t"
    "v_lshlrev_b32 v98, 16, v85\n\t"
    "v_and_b32 v99, 0xffff0000, v85\n\t"
    "v_pk_fma_f32 v[58:59], v[98:99], v[8:9], v[58:59]\n\t"
    "s_waitcnt lgkmcnt(1)\n\t"
    "v_pk_add_f32 v[56:57], v[56:57], v[72:73]\n\t"
    "v_pk_add_f32 v[58:59], v[58:59], v[74:75]\n\t"
    "v_cvt_pk_bf16_f32 v100, v56, v57\n\t"
    "v_cvt_pk_bf16_f32 v101, v58, v59\n\t"
    "buffer_store_dwordx2 v[100:101], v7, s[24:27], s81 offen offset:3072 sc1\n\t"
    "v_lshlrev_b32 v104, 16, v86\n\t"
    "v_and_b32 v105, 0xffff0000, v86\n\t"
    "v_pk_fma_f32 v[60:61], v[104:105], v[8:9], v[60:61]\n\t"
    "v_lshlrev_b32 v106, 16, v87\n\t"
    "v_and_b32 v107, 0xffff0000, v87\n\t"
    "v_pk_fma_f32 v[62:63], v[106:107], v[8:9], v[62:63]\n\t"
    "s_waitcnt lgkmcnt(0)\n\t"
    "v_pk_add_f32 v[60:61], v[60:61], v[76:77]\n\t"
    "v_pk_add_f32 v[62:63], v[62:63], v[78:79]\n\t"
    "v_cvt_pk_bf16_f32 v108, v60, v61\n\t"
    "v_cvt_pk_bf16_f32 v109, v62, v63\n\t"
    "buffer_store_dwordx2 v[108:109], v7, s[24:27], s81 offen offset:3584 sc1\n\t"
    "buffer_load_dwordx4 a[64:67], v24, s[24:27], s40 offen sc1\n\t"
    "buffer_load_dwordx4 a[68:71], v25, s[24:27], s40 offen sc1\n\t"
    "LGM_cj_20:\n\t"
    "; GEMM2: 64 tokens x 64 channels, K split over the 4 waves\n\t"
    "ds_read_b64_tr_b16 v[100:101], v136 offset:0\n\t"
    "ds_read_b64_tr_b16 v[102:103], v136 offset:2048\n\t"
    "ds_read_b64_tr_b16 v[104:105], v137 offset:0\n\t"
    "ds_read_b64_tr_b16 v[106:107], v137 offset:2048\n\t"
    "ds_read_b64_tr_b16 v[108:109], v138 offset:0\n\t"
    "ds_read_b64_tr_b16 v[110:111], v138 offset:2048\n\t"
    "ds_read_b64_tr_b16 v[112:113], v139 offset:0\n\t"
    "ds_read_b64_tr_b16 v[114:115], v139 offset:2048\n\t"
    "s_waitcnt lgkmcnt(0)\n\t"
    "s_waitcnt vmcnt(22)\n\t"
    "v_mfma_f32_16x16x32_bf16 v[192:195], v[100:103], a[0:3], 0\n\t"
    "s_waitcnt vmcnt(21)\n\t"
    "v_mfma_f32_16x16x32_bf16 v[208:211], v[100:103], a[4:7], 0\n\t"
    "ds_read_b64_tr_b16 v[116:117], v136 offset:16384\n\t"
    "buffer_load_dwordx4 a[72:75], v26, s[24:27], s40 offen sc1\n\t"
    "s_waitcnt vmcnt(21)\n\t"
    "v_mfma_f32_16x16x32_bf16 v[224:227], v[100:103], a[8:11], 0\n\t"
    "ds_read_b64_tr_b16 v[118:119], v136 offset:18432\n\t"
    "s_waitcnt vmcnt(19)\n\t"
    "v_mfma_f32_16x16x32_bf16 v[240:243], v[100:103], a[12:15], 0\n\t"
    "ds_read_b64_tr_b16 v[120:121], v137 offset:16384\n\t"
    "v_mfma_f32_16x16x32_bf16 v[196:199], v[104:107], a[0:3], 0\n\t"
    "ds_read_b64_tr_b16 v[122:123], v137 offset:18432\n\t"
    "buffer_load_dwordx4 a[76:79], v27, s[24:27], s40 offen sc1\n\t"
    "v_mfma_f32_16x16x32_bf16 v[212:215], v[104:107], a[4:7], 0\n\t"
    "ds_read_b64_tr_b16 v[124:125], v138 offset:16384\n\t"
    "v_mfma_f32_16x16x32_bf16 v[228:231], v[104:107], a[8:11], 0\n\t"
    "ds_read_b64_tr_b16 v[126:127], v138 offset:18432\n\t"
    "v_mfma_f32_16x16x32_bf16 v[244:247], v[104:107], a[12:15], 0\n\t"
    "ds_read_b64_tr_b16 v[128:129], v139 offset:16384\n\t"
    "buffer_load_dwordx4 a[80:83], v24, s[24:27], s41 offen sc1\n\t"
    "v_mfma_f32_16x16x32_bf16 v[200:203], v[108:111], a[0:3], 0\n\t"
    "ds_read_b64_tr_b16 v[130:131], v139 offset:18432\n\t"
    "v_mfma_f32_16x16x32_bf16 v[216:219], v[108:111], a[4:7], 0\n\t"
    "buffer_load_dwordx4 a[84:87], v25, s[24:27], s41 offen sc1\n\t"
    "v_mfma_f32_16x16x32_bf16 v[232:235], v[108:111], a[8:11], 0\n\t"
    "v_mfma_f32_16x16x32_bf16 v[248:251], v[108:111], a[12:15], 0\n\t"
    "v_mfma_f32_16x16x32_bf16 v[204:207], v[112:115], a[0:3], 0\n\t"
    "buffer_load_dwordx4 a[88:91], v26, s[24:27], s41 offen sc1\n\t"
    "v_mfma_f32_16x16x32_bf16 v[220:223], v[112:115], a[4:7], 0\n\t"
    "v_mfma_f32_16x16x32_bf16 v[236:239], v[112:115], a[8:11], 0\n\t"
    "v_mfma_f32_16x16x32_bf16 v[252:255], v[112:115], a[12:15], 0\n\t"
    "buffer_load_dwordx4 a[92:95], v27, s[24:27], s41 offen sc1\n\t"
    "s_waitcnt lgkmcnt(0)\n\t"
    "s_waitcnt vmcnt(23)\n\t"
    "v_mfma_f32_16x16x32_bf16 v[192:195], v[116:119], a[16:19], v[192:195]\n\t"
    "s_waitcnt vmcnt(22)\n\t"
    "v_mfma_f32_16x16x32_bf16 v[208:211], v[116:119], a[20:23], v[208:211]\n\t"
    "ds_read_b64_tr_b16 v[100:101], v136 offset:32768\n\t"
    "buffer_load_dwordx4 a[96:99], v24, s[24:27], s42 offen sc1\n\t"
    "s_waitcnt vmcnt(22)\n\t"
    "v_mfma_f32_16x16x32_bf16 v[224:227], v[116:119], a[24:27], v[224:227]\n\t"
    "ds_read_b64_tr_b16 v[102:103], v136 offset:34816\n\t"
    "s_waitcnt vmcnt(21)\n\t"
    "v_mfma_f32_16x16x32_bf16 v[240:243], v[116:119], a[28:31], v[240:243]\n\t"
    "ds_read_b64_tr_b16 v[104:105], v137 offset:32768\n\t"
    "buffer_load_dwordx4 a[100:103], v25, s[24:27], s42 offen sc1\n\t"
    "v_mfma_f32_16x16x32_bf16 v[196:199], v[120:123], a[16:19], v[196:199]\n\t"
    "ds_read_b64_tr_b16 v[106:107], v137 offset:34816\n\t"
    "v_mfma_f32_16x16x32_bf16 v[212:215], v[120:123], a[20:23], v[212:215]\n\t"
    "ds_read_b64_tr_b16 v[108:109], v138 offset:32768\n\t"
    "v_mfma_f32_16x16x32_bf16 v[228:231], v[120:123], a[24:27], v[228:231]\n\t"
    "ds_read_b64_tr_b16 v[110:111], v138 offset:34816\n\t"
    "s_waitcnt vmcnt(10)\n\t"
    "s_barrier\n\t"
    "s_cmp_lg_u32 s69, 0\n\t"
    "s_cbranch_scc1 LGM_fl_21\n\t"
    "s_lshl_b32 s78, s68, 0\n\t"
    "s_lshl_b32 s78, s78, 3\n\t"
    "s_add_u32 s78, s78, 12288\n\t"
    "v_mov_b32 v16, s66\n\t"
    "v_mov_b32 v17, s67\n\t"
    "v_mov_b32 v18, 0\n\t"
    "s_mov_b64 exec, 1\n\t"
    "buffer_store_dwordx2 v[16:17], v18, s[24:27], s78 offen sc1\n\t"
    "s_mov_b64 exec, -1\n\t"
    "LGM_fl_21:\n\t"
    "v_mfma_f32_16x16x32_bf16 v[244:247], v[120:123], a[28:31], v[244:247]\n\t"
    "ds_read_b64_tr_b16 v[112:113], v139 offset:32768\n\t"
    "v_mfma_f32_16x16x32_bf16 v[200:203], v[124:127], a[16:19], v[200:203]\n\t"
    "ds_read_b64_tr_b16 v[114:115], v139 offset:34816\n\t"
    "buffer_load_dwordx4 a[104:107], v26, s[24:27], s42 offen sc1\n\t"
    "v_mfma_f32_16x16x32_bf16 v[216:219], v[124:127], a[20:23], v[216:219]\n\t"
    "v_mfma_f32_16x16x32_bf16 v[232:235], v[124:127], a[24:27], v[232:235]\n\t"
    "buffer_load_dwordx4 a[108:111], v27, s[24:27], s42 offen sc1\n\t"
    "v_mfma_f32_16x16x32_bf16 v[248:251], v[124:127], a[28:31], v[248:251]\n\t"
    "v_mfma_f32_16x16x32_bf16 v[204:207], v[128:131], a[16:19], v[204:207]\n\t"
    "v_mfma_f32_16x16x32_bf16 v[220:223], v[128:131], a[20:23], v[220:223]\n\t"
    "buffer_load_dwordx4 a[112:115], v24, s[24:27], s43 offen sc1\n\t"
    "v_mfma_f32_16x16x32_bf16 v[236:239], v[128:131], a[24:27], v[236:239]\n\t"
    "v_mfma_f32_16x16x32_bf16 v[252:255], v[128:131], a[28:31], v[252:255]\n\t"
    "buffer_load_dwordx4 a[116:119], v25, s[24:27], s43 offen sc1\n\t"
    "s_waitcnt lgkmcnt(0)\n\t"
    "v_mfma_f32_16x16x32_bf16 v[192:195], v[100:103], a[32:35], v[192:195]\n\t"
    "v_mfma_f32_16x16x32_bf16 v[208:211], v[100:103], a[36:39], v[208:211]\n\t"
    "ds_read_b64_tr_b16 v[116:117], v136 offset:49152\n\t"
    "s_add_u32 m0, s87, 0\n\t"
    "s_nop 0\n\t"
    "buffer_load_dwordx4 v20, s[20:23], s59 offen offset:0 lds\n\t"
    "v_mfma_f32_16x16x32_bf16 v[224:227], v[100:103], a[40:43], v[224:227]\n\t"
    "ds_read_b64_tr_b16 v[118:119], v136 offset:51200\n\t"
    "v_mfma_f32_16x16x32_bf16 v[240:243], v[100:103], a[44:47], v[240:243]\n\t"
    "ds_read_b64_tr_b16 v[120:121], v137 offset:49152\n\t"
    "v_mfma_f32_16x16x32_bf16 v[196:199], v[104:107], a[32:35], v[196:199]\n\t"
    "ds_read_b64_tr_b16 v[122:123], v137 offset:51200\n\t"
    "buffer_load_dwordx4 v21, s[20:23], s59 offen offset:1024 lds\n\t"
    "v_mfma_f32_16x16x32_bf16 v[212:215], v[104:107], a[36:39], v[212:215]\n\t"
    "ds_read_b64_tr_b16 v[124:125], v138 offset:49152\n\t"
    "v_mfma_f32_16x16x32_bf16 v[228:231], v[104:107], a[40:43], v[228:231]\n\t"
    "ds_read_b64_tr_b16 v[126:127], v138 offset:51200\n\t"
    "v_mfma_f32_16x16x32_bf16 v[244:247], v[104:107], a[44:47], v[244:247]\n\t"
    "ds_read_b64_tr_b16 v[128:129], v139 offset:49152\n\t"
    "buffer_load_dwordx4 v22, s[20:23], s59 offen offset:2048 lds\n\t"
    "v_mfma_f32_16x16x32_bf16 v[200:203], v[108:111], a[32:35], v[200:203]\n\t"
    "ds_read_b64_tr_b16 v[130:131], v139 offset:51200\n\t"
    "v_mfma_f32_16x16x32_bf16 v[216:219], v[108:111], a[36:39], v[216:219]\n\t"
    "buffer_load_dwordx4 v23, s[20:23], s59 offen offset:3072 lds\n\t"
    "v_mfma_f32_16x16x32_bf16 v[232:235], v[108:111], a[40:43], v[232:235]\n\t"
    "v_mfma_f32_16x16x32_bf16 v[248:251], v[108:111], a[44:47], v[248:251]\n\t"
    "v_mfma_f32_16x16x32_bf16 v[204:207], v[112:115], a[32:35], v[204:207]\n\t"
    "buffer_load_dwordx4 a[120:123], v26, s[24:27], s43 offen sc1\n\t"
    "v_mfma_f32_16x16x32_bf16 v[220:223], v[112:115], a[36:39], v[220:223]\n\t"
    "v_mfma_f32_16x16x32_bf16 v[236:239], v[112:115], a[40:43], v[236:239]\n\t"
    "v_mfma_f32_16x16x32_bf16 v[252:255], v[112:115], a[44:47], v[252:255]\n\t"
    "buffer_load_dwordx4 a[124:127], v27, s[24:27], s43 offen sc1\n\t"
    "s_waitcnt lgkmcnt(0)\n\t"
    "v_mfma_f32_16x16x32_bf16 v[192:195], v[116:119], a[48:51], v[192:195]\n\t"
    "v_mfma_f32_16x16x32_bf16 v[208:211], v[116:119], a[52:55], v[208:211]\n\t"
    "ds_read_b64_tr_b16 v[100:101], v140 offset:0\n\t"
    "s_add_u32 m0, s87, 16384\n\t"
    "s_nop 0\n\t"
    "buffer_load_dwordx4 v20, s[20:23], s60 offen offset:0 lds\n\t"
    "v_mfma_f32_16x16x32_bf16 v[224:227], v[116:119], a[56:59], v[224:227]\n\t"
    "ds_read_b64_tr_b16 v[102:103], v140 offset:2048\n\t"
    "v_mfma_f32_16x16x32_bf16 v[240:243], v[116:119], a[60:63], v[240:243]\n\t"
    "ds_read_b64_tr_b16 v[104:105], v141 offset:0\n\t"
    "v_mfma_f32_16x16x32_bf16 v[196:199], v[120:123], a[48:51], v[196:199]\n\t"
    "ds_read_b64_tr_b16 v[106:107], v141 offset:2048\n\t"
    "buffer_load_dwordx4 v21, s[20:23], s60 offen offset:1024 lds\n\t"
    "v_mfma_f32_16x16x32_bf16 v[212:215], v[120:123], a[52:55], v[212:215]\n\t"
    "ds_read_b64_tr_b16 v[108:109], v142 offset:0\n\t"
    "v_mfma_f32_16x16x32_bf16 v[228:231], v[120:123], a[56:59], v[228:231]\n\t"
    "ds_read_b64_tr_b16 v[110:111], v142 offset:2048\n\t"
    "v_mfma_f32_16x16x32_bf16 v[244:247], v[120:123], a[60:63], v[244:247]\n\t"
    "ds_read_b64_tr_b16 v[112:113], v143 offset:0\n\t"
    "buffer_load_dwordx4 v22, s[20:23], s60 offen offset:2048 lds\n\t"
    "v_mfma_f32_16x16x32_bf16 v[200:203], v[124:127], a[48:51], v[200:203]\n\t"
    "ds_read_b64_tr_b16 v[114:115], v143 offset:2048\n\t"
    "v_mfma_f32_16x16x32_bf16 v[216:219], v[124:127], a[52:55], v[216:219]\n\t"
    "buffer_load_dwordx4 v23, s[20:23], s60 offen offset:3072 lds\n\t"
    "v_mfma_f32_16x16x32_bf16 v[232:235], v[124:127], a[56:59], v[232:235]\n\t"
    "v_mfma_f32_16x16x32_bf16 v[248:251], v[124:127], a[60:63], v[248:251]\n\t"
    "v_mfma_f32_16x16x32_bf16 v[204:207], v[128:131], a[48:51], v[204:207]\n\t"
    "buffer_load_dwordx4 a[128:131], v24, s[24:27], s44 offen sc1\n\t"
    "v_mfma_f32_16x16x32_bf16 v[220:223], v[128:131], a[52:55], v[220:223]\n\t"
    "v_mfma_f32_16x16x32_bf16 v[236:239], v[128:131], a[56:59], v[236:239]\n\t"
    "v_mfma_f32_16x16x32_bf16 v[252:255], v[128:131], a[60:63], v[252:255]\n\t"
    "buffer_load_dwordx4 a[132:135], v25, s[24:27], s44 offen sc1\n\t"
    "s_waitcnt lgkmcnt(0)\n\t"
    "s_waitcnt vmcnt(25)\n\t"
    "v_mfma_f32_16x16x32_bf16 v[192:195], v[100:103], a[64:67], v[192:195]\n\t"
    "s_waitcnt vmcnt(24)\n\t"
    "v_mfma_f32_16x16x32_bf16 v[208:211], v[100:103], a[68:71], v[208:211]\n\t"
    "ds_read_b64_tr_b16 v[116:117], v140 offset:16384\n\t"
    "buffer_load_dwordx4 a[136:139], v26, s[24:27], s44 offen sc1\n\t"
    "s_waitcnt vmcnt(24)\n\t"
    "v_mfma_f32_16x16x32_bf16 v[224:227], v[100:103], a[72:75], v[224:227]\n\t"
    "ds_read_b64_tr_b16 v[118:119], v140 offset:18432\n\t"
    "v_lshlrev_b32 v48, 4, v1\n\t"
    "s_add_u32 s92, s69, 1\n\t"
    "s_waitcnt vmcnt(23)\n\t"
    "v_mfma_f32_16x16x32_bf16 v[240:243], v[100:103], a[76:79], v[240:243]\n\t"
    "ds_read_b64_tr_b16 v[120:121], v141 offset:16384\n\t"
    "v_mfma_f32_16x16x32_bf16 v[196:199], v[104:107], a[64:67], v[196:199]\n\t"
    "ds_read_b64_tr_b16 v[122:123], v141 offset:18432\n\t"
    "buffer_load_dwordx4 a[140:143], v27, s[24:27], s44 offen sc1\n\t"
    "v_mfma_f32_16x16x32_bf16 v[212:215], v[104:107], a[68:71], v[212:215]\n\t"
    "ds_read_b64_tr_b16 v[124:125], v142 offset:16384\n\t"
    "s_and_b32 s92, s92, 3\n\t"
    "s_mul_i32 s92, s92, 12288\n\t"
    "v_mfma_f32_16x16x32_bf16 v[228:231], v[104:107], a[72:75], v[228:231]\n\t"
    "ds_read_b64_tr_b16 v[126:127], v142 offset:18432\n\t"
    "v_mfma_f32_16x16x32_bf16 v[244:247], v[104:107], a[76:79], v[244:247]\n\t"
    "ds_read_b64_tr_b16 v[128:129], v143 offset:16384\n\t"
    "s_add_u32 m0, s87, 32768\n\t"
    "s_nop 0\n\t"
    "buffer_load_dwordx4 v20, s[20:23], s61 offen offset:0 lds\n\t"
    "s_add_u32 s92, s92, 114688\n\t"
    "v_add_u32 v52, s92, v48\n\t"
    "v_mfma_f32_16x16x32_bf16 v[200:203], v[108:111], a[64:67], v[200:203]\n\t"
    "ds_read_b64_tr_b16 v[130:131], v143 offset:18432\n\t"
    "v_mfma_f32_16x16x32_bf16 v[216:219], v[108:111], a[68:71], v[216:219]\n\t"
    "buffer_load_dwordx4 v21, s[20:23], s61 offen offset:1024 lds\n\t"
    "v_mfma_f32_16x16x32_bf16 v[232:235], v[108:111], a[72:75], v[232:235]\n\t"
    "s_add_u32 s92, s69, 2\n\t"
    "s_and_b32 s92, s92, 3\n\t"
    "v_mfma_f32_16x16x32_bf16 v[248:251], v[108:111], a[76:79], v[248:251]\n\t"
    "v_mfma_f32_16x16x32_bf16 v[204:207], v[112:115], a[64:67], v[204:207]\n\t"
    "buffer_load_dwordx4 v22, s[20:23], s61 offen offset:2048 lds\n\t"
    "v_mfma_f32_16x16x32_bf16 v[220:223], v[112:115], a[68:71], v[220:223]\n\t"
    "s_mul_i32 s92, s92, 12288\n\t"
    "s_add_u32 s92, s92, 118784\n\t"
    "v_mfma_f32_16x16x32_bf16 v[236:239], v[112:115], a[72:75], v[236:239]\n\t"
    "v_mfma_f32_16x16x32_bf16 v[252:255], v[112:115], a[76:79], v[252:255]\n\t"
    "buffer_load_dwordx4 v23, s[20:23], s61 offen offset:3072 lds\n\t"
    "v_add_u32 v53, s92, v48\n\t"
    "s_add_u32 s92, s69, 3\n\t"
    "s_waitcnt lgkmcnt(0)\n\t"
    "s_waitcnt vmcnt(27)\n\t"
    "v_mfma_f32_16x16x32_bf16 v[192:195], v[116:119], a[80:83], v[192:195]\n\t"
    "s_waitcnt vmcnt(26)\n\t"
    "v_mfma_f32_16x16x32_bf16 v[208:211], v[116:119], a[84:87], v[208:211]\n\t"
    "ds_read_b64_tr_b16 v[100:101], v140 offset:32768\n\t"
    "buffer_load_dwordx4 a[144:147], v24, s[24:27], s45 offen sc1\n\t"
    "s_waitcnt vmcnt(26)\n\t"
    "v_mfma_f32_16x16x32_bf16 v[224:227], v[116:119], a[88:91], v[224:227]\n\t"
    "ds_read_b64_tr_b16 v[102:103], v140 offset:34816\n\t"
    "s_and_b32 s92, s92, 3\n\t"
    "s_mul_i32 s92, s92, 12288\n\t"
    "s_waitcnt vmcnt(25)\n\t"
    "v_mfma_f32_16x16x32_bf16 v[240:243], v[116:119], a[92:95], v[240:243]\n\t"
    "ds_read_b64_tr_b16 v[104:105], v141 offset:32768\n\t"
    "v_mfma_f32_16x16x32_bf16 v[196:199], v[120:123], a[80:83], v[196:199]\n\t"
    "ds_read_b64_tr_b16 v[106:107], v141 offset:34816\n\t"
    "buffer_load_dwordx4 a[148:151], v25, s[24:27], s45 offen sc1\n\t"
    "v_mfma_f32_16x16x32_bf16 v[212:215], v[120:123], a[84:87], v[212:215]\n\t"
    "ds_read_b64_tr_b16 v[108:109], v142 offset:32768\n\t"
    "s_add_u32 s92, s92, 122880\n\t"
    "v_add_u32 v54, s92, v48\n\t"
    "v_mfma_f32_16x16x32_bf16 v[228:231], v[120:123], a[88:91], v[228:231]\n\t"
    "ds_read_b64_tr_b16 v[110:111], v142 offset:34816\n\t"
    "v_mfma_f32_16x16x32_bf16 v[244:247], v[120:123], a[92:95], v[244:247]\n\t"
    "ds_read_b64_tr_b16 v[112:113], v143 offset:32768\n\t"
    "buffer_load_dwordx4 a[152:155], v26, s[24:27], s45 offen sc1\n\t"
    "s_mul_i32 s92, s69, 12288\n\t"
    "s_add_u32 s92, s92, 114688\n\t"
    "v_mfma_f32_16x16x32_bf16 v[200:203], v[124:127], a[80:83], v[200:203]\n\t"
    "ds_read_b64_tr_b16 v[114:115], v143 offset:34816\n\t"
    "v_mfma_f32_16x16x32_bf16 v[216:219], v[124:127], a[84:87], v[216:219]\n\t"
    "buffer_load_dwordx4 a[156:159], v27, s[24:27], s45 offen sc1\n\t"
    "v_mfma_f32_16x16x32_bf16 v[232:235], v[124:127], a[88:91], v[232:235]\n\t"
    "v_add_u32 v56, s92, v48\n\t"
    "v_lshrrev_b32 v48, 3, v1\n\t"
    "v_mfma_f32_16x16x32_bf16 v[248:251], v[124:127], a[92:95], v[248:251]\n\t"
    "v_mfma_f32_16x16x32_bf16 v[204:207], v[128:131], a[80:83], v[204:207]\n\t"
    "buffer_load_dwordx4 a[160:163], v24, s[24:27], s46 offen sc1\n\t"
    "v_mfma_f32_16x16x32_bf16 v[220:223], v[128:131], a[84:87], v[220:223]\n\t"
    "v_and_b32 v49, 7, v1\n\t"
    "v_and_b32 v50, 6, v48\n\t"
    "v_mfma_f32_16x16x32_bf16 v[236:239], v[128:131], a[88:91], v[236:239]\n\t"
    "v_mfma_f32_16x16x32_bf16 v[252:255], v[128:131], a[92:95], v[252:255]\n\t"
    "buffer_load_dwordx4 a[164:167], v25, s[24:27], s46 offen sc1\n\t"
    "v_xor_b32 v50, v50, v49\n\t"
    "v_lshlrev_b32 v50, 4, v50\n\t"
    "s_waitcnt lgkmcnt(0)\n\t"
    "s_waitcnt vmcnt(29)\n\t"
    "v_mfma_f32_16x16x32_bf16 v[192:195], v[100:103], a[96:99], v[192:195]\n\t"
    "s_waitcnt vmcnt(20)\n\t"
    "v_mfma_f32_16x16x32_bf16 v[208:211], v[100:103], a[100:103], v[208:211]\n\t"
    "ds_read_b64_tr_b16 v[116:117], v136 offset:0\n\t"
    "s_add_u32 m0, s87, 49152\n\t"
    "s_nop 0\n\t"
    "buffer_load_dwordx4 v20, s[20:23], s62 offen offset:0 lds\n\t"
    "v_mfma_f32_16x16x32_bf16 v[224:227], v[100:103], a[104:107], v[224:227]\n\t"
    "ds_read_b64_tr_b16 v[118:119], v136 offset:2048\n\t"
    "v_lshl_add_u32 v50, v48, 7, v50\n\t"
    "v_add_u32 v55, s87, v50\n\t"
    "v_mfma_f32_16x16x32_bf16 v[240:243], v[100:103], a[108:111], v[240:243]\n\t"
    "ds_read_b64_tr_b16 v[120:121], v137 offset:0\n\t"
    "v_mfma_f32_16x16x32_bf16 v[196:199], v[104:107], a[96:99], v[196:199]\n\t"
    "ds_read_b64_tr_b16 v[122:123], v137 offset:2048\n\t"
    "buffer_load_dwordx4 v21, s[20:23], s62 offen offset:1024 lds\n\t"
    "v_mfma_f32_16x16x32_bf16 v[212:215], v[104:107], a[100:103], v[212:215]\n\t"
    "ds_read_b64_tr_b16 v[124:125], v138 offset:0\n\t"
    "s_add_u32 s92, s87, 16384\n\t"
    "v_xor_b32 v50, v48, v49\n\t"
    "v_mfma_f32_16x16x32_bf16 v[228:231], v[104:107], a[104:107], v[228:231]\n\t"
    "ds_read_b64_tr_b16 v[126:127], v138 offset:2048\n\t"
    "v_mfma_f32_16x16x32_bf16 v[244:247], v[104:107], a[108:111], v[244:247]\n\t"
    "ds_read_b64_tr_b16 v[128:129], v139 offset:0\n\t"
    "buffer_load_dwordx4 v22, s[20:23], s62 offen offset:2048 lds\n\t"
    "v_lshlrev_b32 v50, 4, v50\n\t"
    "v_lshl_add_u32 v50, v48, 7, v50\n\t"
    "v_mfma_f32_16x16x32_bf16 v[200:203], v[108:111], a[96:99], v[200:203]\n\t"
    "ds_read_b64_tr_b16 v[130:131], v139 offset:2048\n\t"
    "v_mfma_f32_16x16x32_bf16 v[216:219], v[108:111], a[100:103], v[216:219]\n\t"
    "buffer_load_dwordx4 v23, s[20:23], s62 offen offset:3072 lds\n\t"
    "v_mfma_f32_16x16x32_bf16 v[232:235], v[108:111], a[104:107], v[232:235]\n\t"
    "v_add_u32 v62, s92, v50\n\t"
    "v_lshlrev_b32 v50, 4, v49\n\t"
    "v_mfma_f32_16x16x32_bf16 v[248:251], v[108:111], a[108:111], v[248:251]\n\t"
    "v_mfma_f32_16x16x32_bf16 v[204:207], v[112:115], a[96:99], v[204:207]\n\t"
    "buffer_load_dwordx4 a[168:171], v26, s[24:27], s46 offen sc1\n\t"
    "v_mfma_f32_16x16x32_bf16 v[220:223], v[112:115], a[100:103], v[220:223]\n\t"
    "v_lshl_add_u32 v63, v48, 10, v50\n\t"
    "v_lshrrev_b32 v48, 1, v3\n\t"
    "v_mfma_f32_16x16x32_bf16 v[236:239], v[112:115], a[104:107], v[236:239]\n\t"
    "v_mfma_f32_16x16x32_bf16 v[252:255], v[112:115], a[108:111], v[252:255]\n\t"
    "buffer_load_dwordx4 a[172:175], v27, s[24:27], s46 offen sc1\n\t"
    "v_and_b32 v49, 7, v2\n\t"
    "v_lshlrev_b32 v50, 7, v2\n\t"
    "s_waitcnt lgkmcnt(0)\n\t"
    "v_mfma_f32_16x16x32_bf16 v[192:195], v[116:119], a[112:115], v[192:195]\n\t"
    "s_waitcnt vmcnt(20)\n\t"
    "v_mfma_f32_16x16x32_bf16 v[208:211], v[116:119], a[116:119], v[208:211]\n\t"
    "ds_read_b64_tr_b16 v[100:101], v136 offset:16384\n\t"
    "s_add_u32 m0, s87, 65536\n\t"
    "s_nop 0\n\t"
    "buffer_load_dwordx4 v20, s[20:23], s63 offen offset:0 lds\n\t"
    "v_mfma_f32_16x16x32_bf16 v[224:227], v[116:119], a[120:123], v[224:227]\n\t"
    "ds_read_b64_tr_b16 v[102:103], v136 offset:18432\n\t"
    "v_and_b32 v57, 1, v3\n\t"
    "v_lshl_add_u32 v50, v57, 3, v50\n\t"
    "v_mfma_f32_16x16x32_bf16 v[240:243], v[116:119], a[124:127], v[240:243]\n\t"
    "ds_read_b64_tr_b16 v[104:105], v137 offset:16384\n\t"
    "v_mfma_f32_16x16x32_bf16 v[196:199], v[120:123], a[112:115], v[196:199]\n\t"
    "ds_read_b64_tr_b16 v[106:107], v137 offset:18432\n\t"
    "buffer_load_dwordx4 v21, s[20:23], s63 offen offset:1024 lds\n\t"
    "v_mfma_f32_16x16x32_bf16 v[212:215], v[120:123], a[116:119], v[212:215]\n\t"
    "ds_read_b64_tr_b16 v[108:109], v138 offset:16384\n\t"
    "v_add_u32 v50, s92, v50\n\t"
    "v_or_b32 v57, 0, v48\n\t"
    "v_mfma_f32_16x16x32_bf16 v[228:231], v[120:123], a[120:123], v[228:231]\n\t"
    "ds_read_b64_tr_b16 v[110:111], v138 offset:18432\n\t"
    "v_mfma_f32_16x16x32_bf16 v[244:247], v[120:123], a[124:127], v[244:247]\n\t"
    "ds_read_b64_tr_b16 v[112:113], v139 offset:16384\n\t"
    "buffer_load_dwordx4 v22, s[20:23], s63 offen offset:2048 lds\n\t"
    "v_xor_b32 v57, v57, v49\n\t"
    "v_lshl_add_u32 v58, v57, 4, v50\n\t"
    "v_mfma_f32_16x16x32_bf16 v[200:203], v[124:127], a[112:115], v[200:203]\n\t"
    "ds_read_b64_tr_b16 v[114:115], v139 offset:18432\n\t"
    "v_mfma_f32_16x16x32_bf16 v[216:219], v[124:127], a[116:119], v[216:219]\n\t"
    "buffer_load_dwordx4 v23, s[20:23], s63 offen offset:3072 lds\n\t"
    "v_mfma_f32_16x16x32_bf16 v[232:235], v[124:127], a[120:123], v[232:235]\n\t"
    "v_or_b32 v57, 2, v48\n\t"
    "v_xor_b32 v57, v57, v49\n\t"
    "v_mfma_f32_16x16x32_bf16 v[248:251], v[124:127], a[124:127], v[248:251]\n\t"
    "v_mfma_f32_16x16x32_bf16 v[204:207], v[128:131], a[112:115], v[204:207]\n\t"
    "buffer_load_dwordx4 a[176:179], v24, s[24:27], s47 offen sc1\n\t"
    "v_mfma_f32_16x16x32_bf16 v[220:223], v[128:131], a[116:119], v[220:223]\n\t"
    "v_lshl_add_u32 v59, v57, 4, v50\n\t"
    "v_or_b32 v57, 4, v48\n\t"
    "v_mfma_f32_16x16x32_bf16 v[236:239], v[128:131], a[120:123], v[236:239]\n\t"
    "v_mfma_f32_16x16x32_bf16 v[252:255], v[128:131], a[124:127], v[252:255]\n\t"
    "buffer_load_dwordx4 a[180:183], v25, s[24:27], s47 offen sc1\n\t"
    "v_xor_b32 v57, v57, v49\n\t"
    "v_lshl_add_u32 v60, v57, 4, v50\n\t"
    "s_waitcnt lgkmcnt(0)\n\t"
    "s_waitcnt vmcnt(25)\n\t"
    "v_mfma_f32_16x16x32_bf16 v[192:195], v[100:103], a[128:131], v[192:195]\n\t"
    "s_waitcnt vmcnt(18)\n\t"
    "v_mfma_f32_16x16x32_bf16 v[208:211], v[100:103], a[132:135], v[208:211]\n\t"
    "ds_read_b64_tr_b16 v[116:117], v136 offset:32768\n\t"
    "s_add_u32 m0, s87, 98304\n\t"
    "s_nop 0\n\t"
    "buffer_load_dwordx4 v20, s[20:23], s65 offen offset:0 lds\n\t"
    "v_mfma_f32_16x16x32_bf16 v[224:227], v[100:103], a[136:139], v[224:227]\n\t"
    "ds_read_b64_tr_b16 v[118:119], v136 offset:34816\n\t"
    "v_mfma_f32_16x16x32_bf16 v[240:243], v[100:103], a[140:143], v[240:243]\n\t"
    "ds_read_b64_tr_b16 v[120:121], v137 offset:32768\n\t"
    "buffer_load_dwordx4 v21, s[20:23], s65 offen offset:1024 lds\n\t"
    "v_or_b32 v57, 6, v48\n\t"
    "v_xor_b32 v57, v57, v49\n\t"
    "v_mfma_f32_16x16x32_bf16 v[196:199], v[104:107], a[128:131], v[196:199]\n\t"
    "ds_read_b64_tr_b16 v[122:123], v137 offset:34816\n\t"
    "v_mfma_f32_16x16x32_bf16 v[212:215], v[104:107], a[132:135], v[212:215]\n\t"
    "ds_read_b64_tr_b16 v[124:125], v138 offset:32768\n\t"
    "buffer_load_dwordx4 v22, s[20:23], s65 offen offset:2048 lds\n\t"
    "v_mfma_f32_16x16x32_bf16 v[228:231], v[104:107], a[136:139], v[228:231]\n\t"
    "ds_read_b64_tr_b16 v[126:127], v138 offset:34816\n\t"
    "v_lshl_add_u32 v61, v57, 4, v50\n\t"
    "v_lshrrev_b32 v48, 2, v2\n\t"
    "v_mfma_f32_16x16x32_bf16 v[244:247], v[104:107], a[140:143], v[244:247]\n\t"
    "ds_read_b64_tr_b16 v[128:129], v139 offset:32768\n\t"
    "buffer_load_dwordx4 v23, s[20:23], s65 offen offset:3072 lds\n\t"
    "v_mfma_f32_16x16x32_bf16 v[200:203], v[108:111], a[128:131], v[200:203]\n\t"
    "ds_read_b64_tr_b16 v[130:131], v139 offset:34816\n\t"
    "v_mfma_f32_16x16x32_bf16 v[216:219], v[108:111], a[132:135], v[216:219]\n\t"
    "buffer_load_dwordx4 a[184:187], v26, s[24:27], s47 offen sc1\n\t"
    "v_lshl_add_u32 v48, v3, 2, v48\n\t"
    "v_and_b32 v49, 6, v48\n\t"
    "v_mfma_f32_16x16x32_bf16 v[232:235], v[108:111], a[136:139], v[232:235]\n\t"
    "v_mfma_f32_16x16x32_bf16 v[248:251], v[108:111], a[140:143], v[248:251]\n\t"
    "buffer_load_dwordx4 a[188:191], v27, s[24:27], s47 offen sc1\n\t"
    "v_mfma_f32_16x16x32_bf16 v[204:207], v[112:115], a[128:131], v[204:207]\n\t"
    "v_bfe_u32 v50, v1, 1, 1\n\t"
    "s_lshl_b32 s92, s69, 1\n\t"
    "v_mfma_f32_16x16x32_bf16 v[220:223], v[112:115], a[132:135], v[220:223]\n\t"
    "s_or_b32 s78, s68, s69\n\t"
    "s_cmp_lg_u32 s78, 0\n\t"
    "s_cbranch_scc1 LGM_nxp_22\n\t"
    "v_lshlrev_b32 v44, 5, v1\n\t"
    "s_movk_i32 s78, 4096\n\t"
    "buffer_load_dwordx4 v[36:39], v44, s[24:27], s78 offen sc1\n\t"
    "buffer_load_dwordx4 v[40:43], v44, s[24:27], s78 offen offset:16 sc1\n\t"
    "LGM_nxp_22:\n\t"
    "v_mfma_f32_16x16x32_bf16 v[236:239], v[112:115], a[136:139], v[236:239]\n\t"
    "v_mfma_f32_16x16x32_bf16 v[252:255], v[112:115], a[140:143], v[252:255]\n\t"
    "s_lshl_b32 s78, s73, 8\n\t"
    "s_add_u32 s78, s78, 12288\n\t"
    "v_lshlrev_b32 v13, 3, v1\n\t"
    "v_add_u32 v13, s78, v13\n\t"
    "s_lshr_b32 s79, s72, 1\n\t"
    "s_and_b32 s78, s79, 3\n\t"
    "s_lshl_b32 s78, s78, 6\n\t"
    "s_lshr_b32 s79, s79, 2\n\t"
    "s_lshl_b32 s79, s79, 1\n\t"
    "s_add_u32 s78, s78, s79\n\t"
    "s_and_b32 s79, s72, 1\n\t"
    "s_add_u32 s78, s78, s79\n\t"
    "s_lshl_b32 s78, s78, 3\n\t"
    "s_add_u32 s78, s78, 12288\n\t"
    "v_and_b32 v4, 7, v1\n\t"
    "v_lshlrev_b32 v4, 6, v4\n\t"
    "v_add_u32 v4, s78, v4\n\t"
    "v_cmp_gt_u32 vcc, 32, v1\n\t"
    "s_nop 1\n\t"
    "v_cndmask_b32 v13, v4, v13, vcc\n\t"
    "buffer_load_dwordx2 v[14:15], v13, s[24:27], 0 offen sc1\n\t"
    "v_add_u32 v50, s92, v50\n\t"
    "v_xor_b32 v49, v49, v50\n\t"
    "s_waitcnt lgkmcnt(0)\n\t"
    "s_waitcnt vmcnt(24)\n\t"
    "v_mfma_f32_16x16x32_bf16 v[192:195], v[116:119], a[144:147], v[192:195]\n\t"
    "s_waitcnt vmcnt(15)\n\t"
    "v_mfma_f32_16x16x32_bf16 v[208:211], v[116:119], a[148:151], v[208:211]\n\t"
    "ds_read_b64_tr_b16 v[100:101], v136 offset:49152\n\t"
    "s_add_u32 m0, s87, 81920\n\t"
    "s_nop 0\n\t"
    "buffer_load_dwordx4 v20, s[20:23], s64 offen offset:0 lds\n\t"
    "v_mfma_f32_16x16x32_bf16 v[224:227], v[116:119], a[152:155], v[224:227]\n\t"
    "ds_read_b64_tr_b16 v[102:103], v136 offset:51200\n\t"
    "v_mfma_f32_16x16x32_bf16 v[240:243], v[116:119], a[156:159], v[240:243]\n\t"
    "ds_read_b64_tr_b16 v[104:105], v137 offset:49152\n\t"
    "buffer_load_dwordx4 v21, s[20:23], s64 offen offset:1024 lds\n\t"
    "v_lshlrev_b32 v49, 4, v49\n\t"
    "v_lshl_add_u32 v49, v48, 7, v49\n\t"
    "v_mfma_f32_16x16x32_bf16 v[196:199], v[120:123], a[144:147], v[196:199]\n\t"
    "ds_read_b64_tr_b16 v[106:107], v137 offset:51200\n\t"
    "v_mfma_f32_16x16x32_bf16 v[212:215], v[120:123], a[148:151], v[212:215]\n\t"
    "ds_read_b64_tr_b16 v[108:109], v138 offset:49152\n\t"
    "buffer_load_dwordx4 v22, s[20:23], s64 offen offset:2048 lds\n\t"
    "v_mfma_f32_16x16x32_bf16 v[228:231], v[120:123], a[152:155], v[228:231]\n\t"
    "ds_read_b64_tr_b16 v[110:111], v138 offset:51200\n\t"
    "v_and_b32 v50, 1, v1\n\t"
    "v_lshl_add_u32 v57, v50, 3, v49\n\t"
    "v_mfma_f32_16x16x32_bf16 v[244:247], v[120:123], a[156:159], v[244:247]\n\t"
    "ds_read_b64_tr_b16 v[112:113], v139 offset:49152\n\t"
    "buffer_load_dwordx4 v23, s[20:23], s64 offen offset:3072 lds\n\t"
    "v_mfma_f32_16x16x32_bf16 v[200:203], v[124:127], a[144:147], v[200:203]\n\t"
    "ds_read_b64_tr_b16 v[114:115], v139 offset:51200\n\t"
    "v_mfma_f32_16x16x32_bf16 v[216:219], v[124:127], a[148:151], v[216:219]\n\t"
    "buffer_load_dwordx4 a[192:195], v24, s[24:27], s48 offen sc1\n\t"
    "s_lshl_b32 s92, s72, 16\n\t"
    "s_lshl_b32 s93, s69, 14\n\t"
    "v_mfma_f32_16x16x32_bf16 v[232:235], v[124:127], a[152:155], v[232:235]\n\t"
    "v_mfma_f32_16x16x32_bf16 v[248:251], v[124:127], a[156:159], v[248:251]\n\t"
    "buffer_load_dwordx4 a[196:199], v25, s[24:27], s48 offen sc1\n\t"
    "v_mfma_f32_16x16x32_bf16 v[204:207], v[128:131], a[144:147], v[204:207]\n\t"
    "s_add_u32 s92, s92, s93\n\t"
    "s_lshl_b32 s93, s73, 7\n\t"
    "v_mfma_f32_16x16x32_bf16 v[220:223], v[128:131], a[148:151], v[220:223]\n\t"
    "buffer_load_dwordx4 a[200:203], v26, s[24:27], s48 offen sc1\n\t"
    "v_mfma_f32_16x16x32_bf16 v[236:239], v[128:131], a[152:155], v[236:239]\n\t"
    "v_mfma_f32_16x16x32_bf16 v[252:255], v[128:131], a[156:159], v[252:255]\n\t"
    "buffer_load_dwordx4 a[204:207], v27, s[24:27], s48 offen sc1\n\t"
    "s_add_u32 s92, s92, s93\n\t"
    "s_add_u32 s93, s92, 8192\n\t"
    "s_waitcnt lgkmcnt(0)\n\t"
    "v_mfma_f32_16x16x32_bf16 v[192:195], v[100:103], a[160:163], v[192:195]\n\t"
    "s_waitcnt vmcnt(17)\n\t"
    "v_mfma_f32_16x16x32_bf16 v[208:211], v[100:103], a[164:167], v[208:211]\n\t"
    "ds_read_b64_tr_b16 v[116:117], v140 offset:0\n\t"
    "v_mfma_f32_16x16x32_bf16 v[224:227], v[100:103], a[168:171], v[224:227]\n\t"
    "ds_read_b64_tr_b16 v[118:119], v140 offset:2048\n\t"
    "buffer_load_dwordx4 a[208:211], v24, s[24:27], s49 offen sc1\n\t"
    "v_mfma_f32_16x16x32_bf16 v[240:243], v[100:103], a[172:175], v[240:243]\n\t"
    "ds_read_b64_tr_b16 v[120:121], v141 offset:0\n\t"
    "v_mfma_f32_16x16x32_bf16 v[196:199], v[104:107], a[160:163], v[196:199]\n\t"
    "ds_read_b64_tr_b16 v[122:123], v141 offset:2048\n\t"
    "v_mfma_f32_16x16x32_bf16 v[212:215], v[104:107], a[164:167], v[212:215]\n\t"
    "ds_read_b64_tr_b16 v[124:125], v142 offset:0\n\t"
    "buffer_load_dwordx4 a[212:215], v25, s[24:27], s49 offen sc1\n\t"
    "v_mfma_f32_16x16x32_bf16 v[228:231], v[104:107], a[168:171], v[228:231]\n\t"
    "ds_read_b64_tr_b16 v[126:127], v142 offset:2048\n\t"
    "v_mfma_f32_16x16x32_bf16 v[244:247], v[104:107], a[172:175], v[244:247]\n\t"
    "ds_read_b64_tr_b16 v[128:129], v143 offset:0\n\t"
    "v_mfma_f32_16x16x32_bf16 v[200:203], v[108:111], a[160:163], v[200:203]\n\t"
    "ds_read_b64_tr_b16 v[130:131], v143 offset:2048\n\t"
    "buffer_load_dwordx4 a[216:219], v26, s[24:27], s49 offen sc1\n\t"
    "v_mfma_f32_16x16x32_bf16 v[216:219], v[108:111], a[164:167], v[216:219]\n\t"
    "v_mfma_f32_16x16x32_bf16 v[232:235], v[108:111], a[168:171], v[232:235]\n\t"
    "v_mfma_f32_16x16x32_bf16 v[248:251], v[108:111], a[172:175], v[248:251]\n\t"
    "buffer_load_dwordx4 a[220:223], v27, s[24:27], s49 offen sc1\n\t"
    "v_mfma_f32_16x16x32_bf16 v[204:207], v[112:115], a[160:163], v[204:207]\n\t"
    "v_mfma_f32_16x16x32_bf16 v[220:223], v[112:115], a[164:167], v[220:223]\n\t"
    "v_mfma_f32_16x16x32_bf16 v[236:239], v[112:115], a[168:171], v[236:239]\n\t"
    "v_lshrrev_b32 v48, 3, v1\n\t"
    "v_and_b32 v49, 7, v1\n\t"
    "v_lshlrev_b32 v50, 4, v49\n\t"
    "v_lshl_add_u32 v50, v48, 12, v50\n\t"
    "s_lshl_b32 s78, s73, 6\n\t"
    "s_lshl_b32 s79, s69, 4\n\t"
    "s_add_u32 s78, s78, s79\n\t"
    "s_lshl_b32 s78, s78, 12\n\t"
    "s_lshl_b32 s79, s72, 7\n\t"
    "s_add_u32 s78, s78, s79\n\t"
    "s_add_u32 s82, s78, 2162688\n\t"
    "s_add_u32 s83, s82, 32768\n\t"
    "v_lshlrev_b32 v51, 2, v2\n\t"
    "s_lshl_b32 s78, s72, 8\n\t"
    "s_lshl_b32 s79, s69, 6\n\t"
    "s_add_u32 s78, s78, s79\n\t"
    "s_add_u32 s81, s78, 16384\n\t"
    "s_waitcnt vmcnt(12)\n\t"
    "v_cmp_eq_u64 vcc, s[66:67], v[14:15]\n\t"
    "s_nop 1\n\t"
    "s_cmp_eq_u64 vcc, -1\n\t"
    "s_cbranch_scc1 LGM_p3ok_23\n\t"
    "s_mov_b32 s77, 0\n\t"
    "LGM_p3_spin_24:\n\t"
    "buffer_load_dwordx2 v[14:15], v13, s[24:27], 0 offen sc1\n\t"
    "s_waitcnt vmcnt(0)\n\t"
    "v_cmp_eq_u64 vcc, s[66:67], v[14:15]\n\t"
    "s_nop 1\n\t"
    "s_cmp_eq_u64 vcc, -1\n\t"
    "s_cbranch_scc1 LGM_p3_done_25\n\t"
    "s_add_u32 s77, s77, 1\n\t"
    "s_cmp_gt_u32 s77, 20000\n\t"
    "s_cbranch_scc1 LGM_p3_done_25\n\t"
    "s_sleep 1\n\t"
    "s_branch LGM_p3_spin_24\n\t"
    "LGM_p3_done_25:\n\t"
    "LGM_p3ok_23:\n\t"
    "buffer_load_dwordx4 v[64:67], v50, s[24:27], s82 offen sc1\n\t"
    "buffer_load_dwordx4 v[68:71], v50, s[24:27], s83 offen sc1\n\t"
    "buffer_load_dword v72, v51, s[24:27], s81 offen sc1\n\t"
    "v_mfma_f32_16x16x32_bf16 v[252:255], v[112:115], a[172:175], v[252:255]\n\t"
    "s_waitcnt lgkmcnt(0)\n\t"
    "v_mfma_f32_16x16x32_bf16 v[192:195], v[116:119], a[176:179], v[192:195]\n\t"
    "s_waitcnt vmcnt(11)\n\t"
    "ds_read_b64_tr_b16 v[100:101], v140 offset:16384\n\t"
    "v_mfma_f32_16x16x32_bf16 v[208:211], v[116:119], a[180:183], v[208:211]\n\t"
    "ds_read_b64_tr_b16 v[102:103], v140 offset:18432\n\t"
    "v_mfma_f32_16x16x32_bf16 v[224:227], v[116:119], a[184:187], v[224:227]\n\t"
    "ds_read_b64_tr_b16 v[104:105], v141 offset:16384\n\t"
    "v_mfma_f32_16x16x32_bf16 v[240:243], v[116:119], a[188:191], v[240:243]\n\t"
    "ds_read_b64_tr_b16 v[106:107], v141 offset:18432\n\t"
    "ds_read_b64_tr_b16 v[108:109], v142 offset:16384\n\t"
    "v_mfma_f32_16x16x32_bf16 v[196:199], v[120:123], a[176:179], v[196:199]\n\t"
    "ds_read_b64_tr_b16 v[110:111], v142 offset:18432\n\t"
    "v_mfma_f32_16x16x32_bf16 v[212:215], v[120:123], a[180:183], v[212:215]\n\t"
    "ds_read_b64_tr_b16 v[112:113], v143 offset:16384\n\t"
    "v_mfma_f32_16x16x32_bf16 v[228:231], v[120:123], a[184:187], v[228:231]\n\t"
    "ds_read_b64_tr_b16 v[114:115], v143 offset:18432\n\t"
    "v_mfma_f32_16x16x32_bf16 v[244:247], v[120:123], a[188:191], v[244:247]\n\t"
    "ds_read_b64_tr_b16 v[76:77], v140 offset:32768\n\t"
    "v_mfma_f32_16x16x32_bf16 v[200:203], v[124:127], a[176:179], v[200:203]\n\t"
    "ds_read_b64_tr_b16 v[78:79], v140 offset:34816\n\t"
    "v_mfma_f32_16x16x32_bf16 v[216:219], v[124:127], a[180:183], v[216:219]\n\t"
    "ds_read_b64_tr_b16 v[80:81], v141 offset:32768\n\t"
    "v_mfma_f32_16x16x32_bf16 v[232:235], v[124:127], a[184:187], v[232:235]\n\t"
    "ds_read_b64_tr_b16 v[82:83], v141 offset:34816\n\t"
    "ds_read_b64_tr_b16 v[84:85], v142 offset:32768\n\t"
    "v_mfma_f32_16x16x32_bf16 v[248:251], v[124:127], a[188:191], v[248:251]\n\t"
    "ds_read_b64_tr_b16 v[86:87], v142 offset:34816\n\t"
    "v_mfma_f32_16x16x32_bf16 v[204:207], v[128:131], a[176:179], v[204:207]\n\t"
    "ds_read_b64_tr_b16 v[88:89], v143 offset:32768\n\t"
    "v_mfma_f32_16x16x32_bf16 v[220:223], v[128:131], a[180:183], v[220:223]\n\t"
    "ds_read_b64_tr_b16 v[90:91], v143 offset:34816\n\t"
    "v_mfma_f32_16x16x32_bf16 v[236:239], v[128:131], a[184:187], v[236:239]\n\t"
    "v_mfma_f32_16x16x32_bf16 v[252:255], v[128:131], a[188:191], v[252:255]\n\t"
    "s_waitcnt lgkmcnt(0)\n\t"
    "s_waitcnt vmcnt(9)\n\t"
    "v_mfma_f32_16x16x32_bf16 v[208:211], v[100:103], a[196:199], v[208:211]\n\t"
    "v_mfma_f32_16x16x32_bf16 v[212:215], v[104:107], a[196:199], v[212:215]\n\t"
    "v_mfma_f32_16x16x32_bf16 v[216:219], v[108:111], a[196:199], v[216:219]\n\t"
    "v_mfma_f32_16x16x32_bf16 v[220:223], v[112:115], a[196:199], v[220:223]\n\t"
    "s_waitcnt vmcnt(2)\n\t"
    "ds_write_b128 v55, v[64:67]\n\t"
    "v_mfma_f32_16x16x32_bf16 v[208:211], v[76:79], a[212:215], v[208:211]\n\t"
    "s_waitcnt vmcnt(1)\n\t"
    "ds_write_b128 v55, v[68:71] offset:1024\n\t"
    "v_mfma_f32_16x16x32_bf16 v[212:215], v[80:83], a[212:215], v[212:215]\n\t"
    "v_mfma_f32_16x16x32_bf16 v[216:219], v[84:87], a[212:215], v[216:219]\n\t"
    "v_mfma_f32_16x16x32_bf16 v[220:223], v[88:91], a[212:215], v[220:223]\n\t"
    "v_mfma_f32_16x16x32_bf16 v[224:227], v[100:103], a[200:203], v[224:227]\n\t"
    "v_mfma_f32_16x16x32_bf16 v[228:231], v[104:107], a[200:203], v[228:231]\n\t"
    "v_mfma_f32_16x16x32_bf16 v[232:235], v[108:111], a[200:203], v[232:235]\n\t"
    "v_mfma_f32_16x16x32_bf16 v[236:239], v[112:115], a[200:203], v[236:239]\n\t"
    "ds_write_b128 v52, v[208:211] offset:0\n\t"
    "v_mfma_f32_16x16x32_bf16 v[224:227], v[76:79], a[216:219], v[224:227]\n\t"
    "ds_write_b128 v52, v[212:215] offset:1024\n\t"
    "v_mfma_f32_16x16x32_bf16 v[228:231], v[80:83], a[216:219], v[228:231]\n\t"
    "ds_write_b128 v52, v[216:219] offset:2048\n\t"
    "v_mfma_f32_16x16x32_bf16 v[232:235], v[84:87], a[216:219], v[232:235]\n\t"
    "ds_write_b128 v52, v[220:223] offset:3072\n\t"
    "v_mfma_f32_16x16x32_bf16 v[236:239], v[88:91], a[216:219], v[236:239]\n\t"
    "v_mfma_f32_16x16x32_bf16 v[240:243], v[100:103], a[204:207], v[240:243]\n\t"
    "v_mfma_f32_16x16x32_bf16 v[244:247], v[104:107], a[204:207], v[244:247]\n\t"
    "v_mfma_f32_16x16x32_bf16 v[248:251], v[108:111], a[204:207], v[248:251]\n\t"
    "v_mfma_f32_16x16x32_bf16 v[252:255], v[112:115], a[204:207], v[252:255]\n\t"
    "ds_write_b128 v53, v[224:227] offset:0\n\t"
    "v_mfma_f32_16x16x32_bf16 v[240:243], v[76:79], a[220:223], v[240:243]\n\t"
    "ds_write_b128 v53, v[228:231] offset:1024\n\t"
    "v_mfma_f32_16x16x32_bf16 v[244:247], v[80:83], a[220:223], v[244:247]\n\t"
    "ds_write_b128 v53, v[232:235] offset:2048\n\t"
    "v_mfma_f32_16x16x32_bf16 v[248:251], v[84:87], a[220:223], v[248:251]\n\t"
    "ds_write_b128 v53, v[236:239] offset:3072\n\t"
    "v_mfma_f32_16x16x32_bf16 v[252:255], v[88:91], a[220:223], v[252:255]\n\t"
    "v_mfma_f32_16x16x32_bf16 v[192:195], v[100:103], a[192:195], v[192:195]\n\t"
    "v_mfma_f32_16x16x32_bf16 v[196:199], v[104:107], a[192:195], v[196:199]\n\t"
    "v_mfma_f32_16x16x32_bf16 v[200:203], v[108:111], a[192:195], v[200:203]\n\t"
    "v_mfma_f32_16x16x32_bf16 v[204:207], v[112:115], a[192:195], v[204:207]\n\t"
    "ds_write_b128 v54, v[240:243] offset:0\n\t"
    "v_mfma_f32_16x16x32_bf16 v[192:195], v[76:79], a[208:211], v[192:195]\n\t"
    "ds_write_b128 v54, v[244:247] offset:1024\n\t"
    "v_mfma_f32_16x16x32_bf16 v[196:199], v[80:83], a[208:211], v[196:199]\n\t"
    "ds_write_b128 v54, v[248:251] offset:2048\n\t"
    "v_mfma_f32_16x16x32_bf16 v[200:203], v[84:87], a[208:211], v[200:203]\n\t"
    "ds_write_b128 v54, v[252:255] offset:3072\n\t"
    "v_mfma_f32_16x16x32_bf16 v[204:207], v[88:91], a[208:211], v[204:207]\n\t"
    "; final epilogue\n\t"
    "s_or_b32 s78, s68, s69\n\t"
    "s_cmp_lg_u32 s78, 0\n\t"
    "s_cbranch_scc1 LGM_nxf_26\n\t"
    "s_mov_b64 s[94:95], -1\n\t"
    "v_cmp_eq_u64 vcc, s[66:67], v[36:37]\n\t"
    "s_nop 1\n\t"
    "s_and_b64 s[94:95], s[94:95], vcc\n\t"
    "v_cmp_eq_u64 vcc, s[66:67], v[38:39]\n\t"
    "s_nop 1\n\t"
    "s_and_b64 s[94:95], s[94:95], vcc\n\t"
    "v_cmp_eq_u64 vcc, s[66:67], v[40:41]\n\t"
    "s_nop 1\n\t"
    "s_and_b64 s[94:95], s[94:95], vcc\n\t"
    "v_cmp_eq_u64 vcc, s[66:67], v[42:43]\n\t"
    "s_nop 1\n\t"
    "s_and_b64 s[94:95], s[94:95], vcc\n\t"
    "s_cmp_eq_u64 s[94:95], -1\n\t"
    "s_cbranch_scc1 LGM_nxok_27\n\t"
    "v_lshlrev_b32 v44, 3, v1\n\t"
    "v_add_u32 v44, 4096, v44\n\t"
    "s_mov_b32 s77, 0\n\t"
    "LGM_nx0_spin_28:\n\t"
    "buffer_load_dwordx2 v[46:47], v44, s[24:27], 0 offen sc1\n\t"
    "s_waitcnt vmcnt(0)\n\t"
    "v_cmp_eq_u64 vcc, s[66:67], v[46:47]\n\t"
    "s_nop 1\n\t"
    "s_cmp_eq_u64 vcc, -1\n\t"
    "s_cbranch_scc1 LGM_nx0_done_29\n\t"
    "s_add_u32 s77, s77, 1\n\t"
    "s_cmp_gt_u32 s77, 20000\n\t"
    "s_cbranch_scc1 LGM_nx0_done_29\n\t"
    "s_sleep 1\n\t"
    "s_branch LGM_nx0_spin_28\n\t"
    "LGM_nx0_done_29:\n\t"
    "v_lshlrev_b32 v44, 3, v1\n\t"
    "v_add_u32 v44, 4608, v44\n\t"
    "s_mov_b32 s77, 0\n\t"
    "LGM_nx1_spin_30:\n\t"
    "buffer_load_dwordx2 v[46:47], v44, s[24:27], 0 offen sc1\n\t"
    "s_waitcnt vmcnt(0)\n\t"
    "v_cmp_eq_u64 vcc, s[66:67], v[46:47]\n\t"
    "s_nop 1\n\t"
    "s_cmp_eq_u64 vcc, -1\n\t"
    "s_cbranch_scc1 LGM_nx1_done_31\n\t"
    "s_add_u32 s77, s77, 1\n\t"
    "s_cmp_gt_u32 s77, 20000\n\t"
    "s_cbranch_scc1 LGM_nx1_done_31\n\t"
    "s_sleep 1\n\t"
    "s_branch LGM_nx1_spin_30\n\t"
    "LGM_nx1_done_31:\n\t"
    "v_lshlrev_b32 v44, 3, v1\n\t"
    "v_add_u32 v44, 5120, v44\n\t"
    "s_mov_b32 s77, 0\n\t"
    "LGM_nx2_spin_32:\n\t"
    "buffer_load_dwordx2 v[46:47], v44, s[24:27], 0 offen sc1\n\t"
    "s_waitcnt vmcnt(0)\n\t"
    "v_cmp_eq_u64 vcc, s[66:67], v[46:47]\n\t"
    "s_nop 1\n\t"
    "s_cmp_eq_u64 vcc, -1\n\t"
    "s_cbranch_scc1 LGM_nx2_done_33\n\t"
    "s_add_u32 s77, s77, 1\n\t"
    "s_cmp_gt_u32 s77, 20000\n\t"
    "s_cbranch_scc1 LGM_nx2_done_33\n\t"
    "s_sleep 1\n\t"
    "s_branch LGM_nx2_spin_32\n\t"
    "LGM_nx2_done_33:\n\t"
    "v_lshlrev_b32 v44, 3, v1\n\t"
    "v_add_u32 v44, 5632, v44\n\t"
    "s_mov_b32 s77, 0\n\t"
    "LGM_nx3_spin_34:\n\t"
    "buffer_load_dwordx2 v[46:47], v44, s[24:27], 0 offen sc1\n\t"
    "s_waitcnt vmcnt(0)\n\t"
    "v_cmp_eq_u64 vcc, s[66:67], v[46:47]\n\t"
    "s_nop 1\n\t"
    "s_cmp_eq_u64 vcc, -1\n\t"
    "s_cbranch_scc1 LGM_nx3_done_35\n\t"
    "s_add_u32 s77, s77, 1\n\t"
    "s_cmp_gt_u32 s77, 20000\n\t"
    "s_cbranch_scc1 LGM_nx3_done_35\n\t"
    "s_sleep 1\n\t"
    "s_branch LGM_nx3_spin_34\n\t"
    "LGM_nx3_done_35:\n\t"
    "LGM_nxok_27:\n\t"
    "v_mov_b32 v44, s66\n\t"
    "v_mov_b32 v45, s67\n\t"
    "v_mov_b32 v46, 0\n\t"
    "s_mov_b64 exec, 1\n\t"
    "buffer_store_dwordx2 v[44:45], v46, s[24:27], 0 offen sc1\n\t"
    "s_mov_b64 exec, -1\n\t"
    "LGM_nxf_26:\n\t"
    "s_waitcnt lgkmcnt(0)\n\t"
    "s_barrier\n\t"
    "ds_read_b128 v[100:103], v56 offset:0\n\t"
    "ds_read_b128 v[116:119], v56 offset:4096\n\t"
    "ds_read_b128 v[132:135], v56 offset:8192\n\t"
    "ds_read_b64_tr_b16 v[80:81], v57 offset:0\n\t"
    "ds_read_b128 v[104:107], v56 offset:1024\n\t"
    "ds_read_b128 v[120:123], v56 offset:5120\n\t"
    "ds_read_b128 v[136:139], v56 offset:9216\n\t"
    "ds_read_b64_tr_b16 v[82:83], v57 offset:4096\n\t"
    "ds_read_b128 v[108:111], v56 offset:2048\n\t"
    "ds_read_b128 v[124:127], v56 offset:6144\n\t"
    "ds_read_b128 v[140:143], v56 offset:10240\n\t"
    "ds_read_b64_tr_b16 v[84:85], v57 offset:8192\n\t"
    "ds_read_b128 v[112:115], v56 offset:3072\n\t"
    "ds_read_b128 v[128:131], v56 offset:7168\n\t"
    "ds_read_b128 v[144:147], v56 offset:11264\n\t"
    "ds_read_b64_tr_b16 v[86:87], v57 offset:12288\n\t"
    "s_waitcnt vmcnt(0)\n\t"
    "v_sub_f32 v74, 1.0, v72\n\t"
    "v_mov_b32 v73, v72\n\t"
    "v_cvt_pk_bf16_f32 v74, v74, v74\n\t"
    "v_lshlrev_b32 v74, 16, v74\n\t"
    "v_mov_b32 v75, v74\n\t"
    "s_waitcnt lgkmcnt(14)\n\t"
    "v_pk_add_f32 v[100:101], v[100:101], v[116:117]\n\t"
    "s_waitcnt lgkmcnt(13)\n\t"
    "v_pk_add_f32 v[192:193], v[192:193], v[132:133]\n\t"
    "v_pk_add_f32 v[102:103], v[102:103], v[118:119]\n\t"
    "v_pk_add_f32 v[194:195], v[194:195], v[134:135]\n\t"
    "s_waitcnt lgkmcnt(12)\n\t"
    "v_lshlrev_b32 v90, 16, v80\n\t"
    "v_pk_add_f32 v[192:193], v[192:193], v[100:101]\n\t"
    "v_and_b32 v91, 0xffff0000, v80\n\t"
    "v_lshlrev_b32 v92, 16, v81\n\t"
    "v_pk_add_f32 v[194:195], v[194:195], v[102:103]\n\t"
    "v_and_b32 v93, 0xffff0000, v81\n\t"
    "v_pk_mul_f32 v[90:91], v[90:91], v[72:73]\n\t"
    "v_pk_mul_f32 v[92:93], v[92:93], v[72:73]\n\t"
    "v_pk_fma_f32 v[192:193], v[192:193], v[74:75], v[90:91]\n\t"
    "v_pk_fma_f32 v[194:195], v[194:195], v[74:75], v[92:93]\n\t"
    "v_cvt_pk_bf16_f32 v94, v192, v193\n\t"
    "v_cvt_pk_bf16_f32 v95, v194, v195\n\t"
    "ds_write_b64 v58, v[94:95]\n\t"
    "s_waitcnt lgkmcnt(11)\n\t"
    "v_pk_add_f32 v[104:105], v[104:105], v[120:121]\n\t"
    "s_waitcnt lgkmcnt(10)\n\t"
    "v_pk_add_f32 v[196:197], v[196:197], v[136:137]\n\t"
    "v_pk_add_f32 v[106:107], v[106:107], v[122:123]\n\t"
    "v_pk_add_f32 v[198:199], v[198:199], v[138:139]\n\t"
    "s_waitcnt lgkmcnt(9)\n\t"
    "v_lshlrev_b32 v90, 16, v82\n\t"
    "v_pk_add_f32 v[196:197], v[196:197], v[104:105]\n\t"
    "v_and_b32 v91, 0xffff0000, v82\n\t"
    "v_lshlrev_b32 v92, 16, v83\n\t"
    "v_pk_add_f32 v[198:199], v[198:199], v[106:107]\n\t"
    "v_and_b32 v93, 0xffff0000, v83\n\t"
    "v_pk_mul_f32 v[90:91], v[90:91], v[72:73]\n\t"
    "v_pk_mul_f32 v[92:93], v[92:93], v[72:73]\n\t"
    "v_pk_fma_f32 v[196:197], v[196:197], v[74:75], v[90:91]\n\t"
    "v_pk_fma_f32 v[198:199], v[198:199], v[74:75], v[92:93]\n\t"
    "v_cvt_pk_bf16_f32 v96, v196, v197\n\t"
    "v_cvt_pk_bf16_f32 v97, v198, v199\n\t"
    "ds_write_b64 v59, v[96:97]\n\t"
    "s_waitcnt lgkmcnt(8)\n\t"
    "v_pk_add_f32 v[108:109], v[108:109], v[124:125]\n\t"
    "s_waitcnt lgkmcnt(7)\n\t"
    "v_pk_add_f32 v[200:201], v[200:201], v[140:141]\n\t"
    "v_pk_add_f32 v[110:111], v[110:111], v[126:127]\n\t"
    "v_pk_add_f32 v[202:203], v[202:203], v[142:143]\n\t"
    "s_waitcnt lgkmcnt(6)\n\t"
    "v_lshlrev_b32 v90, 16, v84\n\t"
    "v_pk_add_f32 v[200:201], v[200:201], v[108:109]\n\t"
    "v_and_b32 v91, 0xffff0000, v84\n\t"
    "v_lshlrev_b32 v92, 16, v85\n\t"
    "v_pk_add_f32 v[202:203], v[202:203], v[110:111]\n\t"
    "v_and_b32 v93, 0xffff0000, v85\n\t"
    "v_pk_mul_f32 v[90:91], v[90:91], v[72:73]\n\t"
    "v_pk_mul_f32 v[92:93], v[92:93], v[72:73]\n\t"
    "v_pk_fma_f32 v[200:201], v[200:201], v[74:75], v[90:91]\n\t"
    "v_pk_fma_f32 v[202:203], v[202:203], v[74:75], v[92:93]\n\t"
    "v_cvt_pk_bf16_f32 v94, v200, v201\n\t"
    "v_cvt_pk_bf16_f32 v95, v202, v203\n\t"
    "ds_write_b64 v60, v[94:95]\n\t"
    "s_waitcnt lgkmcnt(5)\n\t"
    "v_pk_add_f32 v[112:113], v[112:113], v[128:129]\n\t"
    "s_waitcnt lgkmcnt(4)\n\t"
    "v_pk_add_f32 v[204:205], v[204:205], v[144:145]\n\t"
    "v_pk_add_f32 v[114:115], v[114:115], v[130:131]\n\t"
    "v_pk_add_f32 v[206:207], v[206:207], v[146:147]\n\t"
    "s_waitcnt lgkmcnt(3)\n\t"
    "v_lshlrev_b32 v90, 16, v86\n\t"
    "v_pk_add_f32 v[204:205], v[204:205], v[112:113]\n\t"
    "v_and_b32 v91, 0xffff0000, v86\n\t"
    "v_lshlrev_b32 v92, 16, v87\n\t"
    "v_pk_add_f32 v[206:207], v[206:207], v[114:115]\n\t"
    "v_and_b32 v93, 0xffff0000, v87\n\t"
    "v_pk_mul_f32 v[90:91], v[90:91], v[72:73]\n\t"
    "v_pk_mul_f32 v[92:93], v[92:93], v[72:73]\n\t"
    "v_pk_fma_f32 v[204:205], v[204:205], v[74:75], v[90:91]\n\t"
    "v_pk_fma_f32 v[206:207], v[206:207], v[74:75], v[92:93]\n\t"
    "v_cvt_pk_bf16_f32 v96, v204, v205\n\t"
    "v_cvt_pk_bf16_f32 v97, v206, v207\n\t"
    "ds_write_b64 v61, v[96:97]\n\t"
    "s_waitcnt lgkmcnt(0)\n\t"
    "ds_read_b128 v[100:103], v62\n\t"
    "ds_read_b128 v[104:107], v62 offset:1024\n\t"
    "s_waitcnt lgkmcnt(1)\n\t"
    "buffer_store_dwordx4 v[100:103], v63, s[28:31], s92 offen\n\t"
    "s_waitcnt lgkmcnt(0)\n\t"
    "buffer_store_dwordx4 v[104:107], v63, s[28:31], s93 offen\n\t"
    "s_waitcnt vmcnt(0)\n\t"
    : "+v"(tid) : "s"(karg), "s"(bid)
    : "memory", "vcc", "scc",
      "v1", "v2", "v3", "v4", "v5", "v6", "v7", "v8", "v9", "v10", "v11", "v12", "v13", "v14", "v15", "v16",
      "v17", "v18", "v19", "v20", "v21", "v22", "v23", "v24", "v25", "v26", "v27", "v28", "v29", "v30", "v31", "v32",
      "v33", "v34", "v35", "v36", "v37", "v38", "v39", "v40", "v41", "v42", "v43", "v44", "v45", "v46", "v47", "v48",
      "v49", "v50", "v51", "v52", "v53", "v54", "v55", "v56", "v57", "v58", "v59", "v60", "v61", "v62", "v63", "v64",
      "v65", "v66", "v67", "v68", "v69", "v70", "v71", "v72", "v73", "v74", "v75", "v76", "v77", "v78", "v79", "v80",
      "v81", "v82", "v83", "v84", "v85", "v86", "v87", "v88", "v89", "v90", "v91", "v92", "v93", "v94", "v95", "v96",
      "v97", "v98", "v99", "v100", "v101", "v102", "v103", "v104", "v105", "v106", "v107", "v108", "v109", "v110", "v111", "v112",
      "v113", "v114", "v115", "v116", "v117", "v118", "v119", "v120", "v121", "v122", "v123", "v124", "v125", "v126", "v127", "v128",
      "v129", "v130", "v131", "v132", "v133", "v134", "v135", "v136", "v137", "v138", "v139", "v140", "v141", "v142", "v143", "v144",
      "v145", "v146", "v147", "v148", "v149", "v150", "v151", "v152", "v153", "v154", "v155", "v156", "v157", "v158", "v159", "v160",
      "v161", "v162", "v163", "v164", "v165", "v166", "v167", "v168", "v169", "v170", "v171", "v172", "v173", "v174", "v175", "v176",
      "v177", "v178", "v179", "v180", "v181", "v182", "v183", "v184", "v185", "v186", "v187", "v188", "v189", "v190", "v191", "v192",
      "v193", "v194", "v195", "v196", "v197", "v198", "v199", "v200", "v201", "v202", "v203", "v204", "v205", "v206", "v207", "v208",
      "v209", "v210", "v211", "v212", "v213", "v214", "v215", "v216", "v217", "v218", "v219", "v220", "v221", "v222", "v223", "v224",
      "v225", "v226", "v227", "v228", "v229", "v230", "v231", "v232", "v233", "v234", "v235", "v236", "v237", "v238", "v239", "v240",
      "v241", "v242", "v243", "v244", "v245", "v246", "v247", "v248", "v249", "v250", "v251", "v252", "v253", "v254", "v255",
      "a0", "a1", "a2", "a3", "a4", "a5", "a6", "a7", "a8", "a9", "a10", "a11", "a12", "a13", "a14", "a15",
      "a16", "a17", "a18", "a19", "a20", "a21", "a22", "a23", "a24", "a25", "a26", "a27", "a28", "a29", "a30", "a31",
      "a32", "a33", "a34", "a35", "a36", "a37", "a38", "a39", "a40", "a41", "a42", "a43", "a44", "a45", "a46", "a47",
      "a48", "a49", "a50", "a51", "a52", "a53", "a54", "a55", "a56", "a57", "a58", "a59", "a60", "a61", "a62", "a63",
      "a64", "a65", "a66", "a67", "a68", "a69", "a70", "a71", "a72", "a73", "a74", "a75", "a76", "a77", "a78", "a79",
      "a80", "a81", "a82", "a83", "a84", "a85", "a86", "a87", "a88", "a89", "a90", "a91", "a92", "a93", "a94", "a95",
      "a96", "a97", "a98", "a99", "a100", "a101", "a102", "a103", "a104", "a105", "a106", "a107", "a108", "a109", "a110", "a111",
      "a112", "a113", "a114", "a115", "a116", "a117", "a118", "a119", "a120", "a121", "a122", "a123", "a124", "a125", "a126", "a127",
      "a128", "a129", "a130", "a131", "a132", "a133", "a134", "a135", "a136", "a137", "a138", "a139", "a140", "a141", "a142", "a143",
      "a144", "a145", "a146", "a147", "a148", "a149", "a150", "a151", "a152", "a153", "a154", "a155", "a156", "a157", "a158", "a159",
      "a160", "a161", "a162", "a163", "a164", "a165", "a166", "a167", "a168", "a169", "a170", "a171", "a172", "a173", "a174", "a175",
      "a176", "a177", "a178", "a179", "a180", "a181", "a182", "a183", "a184", "a185", "a186", "a187", "a188", "a189", "a190", "a191",
      "a192", "a193", "a194", "a195", "a196", "a197", "a198", "a199", "a200", "a201", "a202", "a203", "a204", "a205", "a206", "a207",
      "a208", "a209", "a210", "a211", "a212", "a213", "a214", "a215", "a216", "a217", "a218", "a219", "a220", "a221", "a222", "a223",
      "a224", "a225", "a226", "a227", "a228", "a229", "a230", "a231", "a232", "a233", "a234", "a235", "a236", "a237", "a238", "a239",
      "a240", "a241", "a242", "a243", "a244", "a245", "a246", "a247", "a248", "a249", "a250", "a251", "a252", "a253", "a254", "a255",
      "s4", "s5", "s6", "s7", "s8", "s9", "s10", "s11", "s12", "s13", "s14", "s15", "s16", "s17", "s18", "s19",
      "s20", "s21", "s22", "s23", "s24", "s25", "s26", "s27", "s28", "s29", "s30", "s31",
      "s34", "s35", "s36", "s37", "s38", "s39", "s40", "s41", "s42", "s43", "s44", "s45", "s46", "s47", "s48", "s49",
      "s50", "s51", "s52", "s53", "s54", "s55", "s56", "s57", "s58", "s59", "s60", "s61", "s62", "s63", "s64", "s65",
      "s66", "s67", "s68", "s69", "s70", "s71", "s72", "s73", "s74", "s75", "s76", "s77", "s78", "s79", "s80", "s81",
      "s82", "s83", "s84", "s85", "s86", "s87", "s88", "s89", "s90", "s91", "s92", "s93", "s94", "s95", "s96", "s97",
      "s98", "s99"
  );
}

extern "C" void kernel_launch(void* const* d_in, const int* in_sizes, int n_in,
                              void* d_out, int out_size, void* d_ws, size_t ws_size,
                              hipStream_t stream) {
  (void)in_sizes; (void)n_in; (void)out_size; (void)ws_size;
  Args a;
  const uint32_t W3 = 0x00020000u;
  a.s[0] = Srd{d_in[0], 2097152u, W3};
  a.s[1] = Srd{d_in[10], 1835008u, W3};
  a.s[2] = Srd{d_in[11], 1835008u, W3};
  a.s[3] = Srd{d_ws, 11599872u, W3};
  a.s[4] = Srd{d_out, 2097152u, W3};
  a.s[5] = Srd{d_in[12], 1024u, W3};
  for (int i = 0; i < 6; ++i) a.s[6 + i] = Srd{d_in[1 + i], 131072u, W3};
  a.logdt = d_in[7];
  a.dskip = d_in[8];
  (void)hipFuncSetAttribute((const void*)gated_mix_kernel, hipFuncAttributeMaxDynamicSharedMemorySize, 163840);
  gated_mix_kernel<<<256, 256, 163840, stream>>>(a);
}
```
